```python
import torch

def _softmax(x, axis=-1):
    return torch.softmax(x, dim=axis)

def _gelu(x):
    return torch.nn.functional.gelu(x, approximate='tanh')

def _silu(x):
    return torch.nn.functional.silu(x)

def _sigmoid(x):
    return torch.sigmoid(x)

def _layernorm(x, axis=-1, eps=1e-05):
    m = x.mean(dim=axis, keepdim=True)
    v = x.var(dim=axis, keepdim=True, unbiased=False)
    return (x - m) / torch.sqrt(v + eps)

def _scatter_add(tgt, idx, src, axis=0):
    return tgt.clone().index_add_(axis, idx, src)

def _segment_sum(src, seg, n):
    out = torch.zeros((n,) + src.shape[1:], dtype=src.dtype)
    return out.index_add_(0, seg, src)

def _pad(x, pw):
    flat = tuple((v for pair in reversed(pw) for v in pair))
    return torch.nn.functional.pad(x, flat)
kernel_name = 'interleave_layers__linear_attn_block__memory_aug_recurrent_mem_tr__prompt'

def setup_inputs():

    _w = lambda a: (lambda t: t.to(torch.bfloat16) if t.is_floating_point() else t)(torch.as_tensor(a))

    def _A_setup():
        B, S, D = (1, 2048, 512)
        return {'x': _w((lambda *s: torch.randn(*s, dtype=torch.bfloat16) * 0.05)(B, S, D)), 'wqkv': _w((lambda *s: torch.randn(*s, dtype=torch.bfloat16) * 0.05)(D, 3 * D)), 'wo': _w((lambda *s: torch.randn(*s, dtype=torch.bfloat16) * 0.05)(D, D))}
    _A_in = _A_setup()

    def _B_setup():
        B, S, Dm = (1, 2048, 512)
        H, Dh = (32, 128)
        M, seg = (8, 32)
        seg = min(seg, S)
        S = S // seg * seg
        return {'x': _w((lambda *s: torch.randn(*s, dtype=torch.bfloat16) * 0.05)(B, S, Dm)), 'mem0': _w((lambda *s: torch.randn(*s, dtype=torch.bfloat16) * 0.4)(B, M, Dm)), 'Wq': _w((lambda *s: torch.randn(*s, dtype=torch.bfloat16) * 3.2)(Dm, H, Dh) * Dm ** (-0.5)), 'Wk': _w((lambda *s: torch.randn(*s, dtype=torch.bfloat16) * 3.2)(Dm, H, Dh) * Dm ** (-0.5)), 'Wv': _w((lambda *s: torch.randn(*s, dtype=torch.bfloat16) * 0.4)(Dm, H, Dh) * Dm ** (-0.5)), 'Wo': _w((lambda *s: torch.randn(*s, dtype=torch.bfloat16) * 0.4)(H, Dh, Dm) * (H * Dh) ** (-0.5)), 'seg_len': seg}
    _B_in = _B_setup()
    return {'A_x': _A_in['x'], 'A_wqkv': _A_in['wqkv'], 'A_wo': _A_in['wo'], 'B_x': _B_in['x'], 'B_mem0': _B_in['mem0'], 'B_Wq': _B_in['Wq'], 'B_Wk': _B_in['Wk'], 'B_Wv': _B_in['Wv'], 'B_Wo': _B_in['Wo'], 'B_seg_len': _B_in['seg_len']}

def reference(A_x, A_wqkv, A_wo, B_x, B_mem0, B_Wq, B_Wk, B_Wv, B_Wo, B_seg_len):

    def _A_ref(x, wqkv, wo):
        B, S, D = x.shape
        H = 32
        Dh = D // H
        qkv = (lambda x, *d: x.permute(*d))((x @ wqkv).reshape(B, S, 3, H, Dh), 2, 0, 3, 1, 4)
        q, k, v = (qkv[0], qkv[1], qkv[2])
        phi = lambda t: torch.where(t > 0, t + 1, torch.exp(t)) + 1e-06
        pq, pk = (phi(q), phi(k))
        kv = torch.einsum('bhsd,bhse->bhde', pk, v)
        z = torch.einsum('bhsd->bhd', pk)
        num = torch.einsum('bhqd,bhde->bhqe', pq, kv)
        den = torch.einsum('bhqd,bhd->bhq', pq, z)[..., None] + 1e-06
        o = num / den
        return (lambda x, *d: x.permute(*d))(o, 0, 2, 1, 3).reshape(B, S, D) @ wo

    def _B_ref(x, mem0, Wq, Wk, Wv, Wo, seg_len):
        B, S, Dm = x.shape
        H, Dh = (Wq.shape[1], Wq.shape[2])
        M = mem0.shape[1]
        scale = Dh ** (-0.5)
        n_seg = S // seg_len
        L = M + seg_len
        idx = torch.arange(L)
        mask = (idx[:, None] < M) | (idx[:, None] >= idx[None, :])
        mem = mem0
        outs = []
        for s in range(n_seg):
            xs = x[:, s * seg_len:(s + 1) * seg_len, :].clone()
            h = torch.concatenate([mem, xs], axis=1)
            q = torch.einsum('btd,dhe->bhte', h, Wq)
            k = torch.einsum('btd,dhe->bhte', h, Wk)
            v = torch.einsum('btd,dhe->bhte', h, Wv)
            scores = torch.einsum('bhqe,bhke->bhqk', q, k) * scale
            scores = torch.where(mask[None, None], scores, -1000000000.0)
            p = _softmax(scores, axis=-1)
            o = torch.einsum('bhqk,bhke->bhqe', p, v)
            o = torch.einsum('bhte,hed->btd', o, Wo)
            mem = o[:, :M, :].clone()
            outs.append(o[:, M:, :])
        return torch.concatenate(outs, axis=1)
    x = A_x
    x = x + _A_ref(_layernorm(x, axis=-1), A_wqkv, A_wo)
    x = x + _B_ref(_layernorm(x, axis=-1), B_mem0, B_Wq, B_Wk, B_Wv, B_Wo, B_seg_len)
    x = x + _A_ref(_layernorm(x, axis=-1), A_wqkv, A_wo)
    x = x + _B_ref(_layernorm(x, axis=-1), B_mem0, B_Wq, B_Wk, B_Wv, B_Wo, B_seg_len)
    return x
```

```cpp
#define OUTPOL 0
#include <hip/hip_runtime.h>
#include <hip/hip_bf16.h>

typedef unsigned short u16;
typedef unsigned u32;
typedef unsigned long long u64;
typedef __bf16 bf16x2 __attribute__((ext_vector_type(2)));
typedef __bf16 bf16x8 __attribute__((ext_vector_type(8)));
typedef __bf16 bf16x16 __attribute__((ext_vector_type(16)));
typedef short s16x4 __attribute__((ext_vector_type(4)));
typedef float f32x2 __attribute__((ext_vector_type(2)));
typedef float f32x4 __attribute__((ext_vector_type(4)));
typedef u32 u32x2 __attribute__((ext_vector_type(2)));
typedef u32 u32x4 __attribute__((ext_vector_type(4)));
typedef u32 u32x8 __attribute__((ext_vector_type(8)));
typedef __amdgpu_buffer_rsrc_t rsrc_t;
#define DI __device__ __forceinline__
#define LAS __attribute__((address_space(3)))
#define GAS __attribute__((address_space(1)))
#define BC(T, x) __builtin_bit_cast(T, x)
#define SB() __builtin_amdgcn_sched_barrier(0)
constexpr int SC1 = 16;
DI u32 f2u(float x) { return __builtin_bit_cast(u32, x); }
DI float u2f(u32 x) { return __builtin_bit_cast(float, x); }

DI u32 pk2(float a, float b) { f32x2 f = {a, b}; return BC(u32, __builtin_convertvector(f, bf16x2)); }
DI u32x2 pk4(f32x4 v) { return (u32x2){pk2(v[0], v[1]), pk2(v[2], v[3])}; }
DI f32x4 mfma32(u32x4 a, u32x4 b, f32x4 c) { return __builtin_amdgcn_mfma_f32_16x16x32_bf16(BC(bf16x8, a), BC(bf16x8, b), c, 0, 0, 0); }
DI f32x4 mfma16(u32x2 a, u32x2 b, f32x4 c) { return __builtin_amdgcn_mfma_f32_16x16x16bf16_1k(BC(s16x4, a), BC(s16x4, b), c, 0, 0, 0); }
DI f32x4 smfma(u32x4 a, u32x8 b, f32x4 c, int idx) { return __builtin_amdgcn_smfmac_f32_16x16x64_bf16(BC(bf16x8, a), BC(bf16x16, b), c, idx, 0, 0); }
DI u32x2 trd(LAS char* p) { return BC(u32x2, __builtin_amdgcn_ds_read_tr16_b64_v4i16((LAS s16x4*)p)); }
DI u32x4 cat(u32x2 a, u32x2 b) { return (u32x4){a[0], a[1], b[0], b[1]}; }
DI u32x8 cat8(u32x4 a, u32x4 b) { return (u32x8){a[0], a[1], a[2], a[3], b[0], b[1], b[2], b[3]}; }
DI rsrc_t mkrsrc(const void* p, u32 n) { return __builtin_amdgcn_make_buffer_rsrc((void*)p, 0, n, 0x00020000); }
DI u32x4 ld16(rsrc_t r, u32 off, u32 soff = 0) { return __builtin_amdgcn_raw_buffer_load_b128(r, off, soff, SC1); }
DI u32x2 ld8(rsrc_t r, u32 off, u32 soff = 0) { return __builtin_amdgcn_raw_buffer_load_b64(r, off, soff, SC1); }
DI u32 ld4(rsrc_t r, u32 off, u32 soff = 0) { return __builtin_amdgcn_raw_buffer_load_b32(r, off, soff, SC1); }
DI u32x4 ld16p(rsrc_t r, u32 off, u32 soff = 0) { return __builtin_amdgcn_raw_buffer_load_b128(r, off, soff, 0); }
DI u32x2 ld8p(rsrc_t r, u32 off, u32 soff = 0) { return __builtin_amdgcn_raw_buffer_load_b64(r, off, soff, 0); }
DI void st16(rsrc_t r, u32 off, u32x4 v, u32 soff = 0) { __builtin_amdgcn_raw_buffer_store_b128(v, r, off, soff, SC1); }
DI void st8(rsrc_t r, u32 off, u32x2 v, u32 soff = 0) { __builtin_amdgcn_raw_buffer_store_b64(v, r, off, soff, SC1); }
DI void st4(rsrc_t r, u32 off, u32 v, u32 soff = 0) { __builtin_amdgcn_raw_buffer_store_b32(v, r, off, soff, SC1); }
DI void bar() { asm volatile("s_waitcnt lgkmcnt(0)\n\ts_barrier" ::: "memory"); }
DI float bflo(u32 x) { return u2f(x << 16); }
DI float bfhi(u32 x) { return u2f(x & 0xffff0000u); }
DI float dot2(u32 a, u32 b, float c) { return __builtin_amdgcn_fdot2_f32_bf16(BC(bf16x2, a), BC(bf16x2, b), c, false); }
DI float xsum(float x) {
  auto a = __builtin_amdgcn_permlane16_swap(f2u(x), f2u(x), false, false); x = u2f(a[0]) + u2f(a[1]);
  auto b = __builtin_amdgcn_permlane32_swap(f2u(x), f2u(x), false, false); return u2f(b[0]) + u2f(b[1]);
}
DI float xmax(float x) {
  auto a = __builtin_amdgcn_permlane16_swap(f2u(x), f2u(x), false, false); x = fmaxf(u2f(a[0]), u2f(a[1]));
  auto b = __builtin_amdgcn_permlane32_swap(f2u(x), f2u(x), false, false); return fmaxf(u2f(b[0]), u2f(b[1]));
}
DI f32x4 phadd(f32x4 a, f32x4 b) {
  f32x4 o;
#pragma unroll
  for (int r = 0; r < 4; ++r) { auto s = __builtin_amdgcn_permlane32_swap(f2u(a[r]), f2u(b[r]), false, false); o[r] = u2f(s[0]) + u2f(s[1]); }
  return o;
}
DI u32 max3u(u32 a, u32 b, u32 c) { return max(max(a, b), c); }
DI u32 nosent(u32 x) { return min(x, 0xfffffffeu); }
constexpr f32x4 Z4 = {0.f, 0.f, 0.f, 0.f};
constexpr u32 ONE2 = 0x3f803f80u, HALF2 = 0x3f003f00u;

constexpr u32 MB = 1u << 20;
constexpr u32 W_COMM = 0;
constexpr u32 W_FLG = 2 * MB;
constexpr u32 F_KVM = W_FLG, F_OA = W_FLG + 4096, F_PAR = W_FLG + 12288, F_OB = W_FLG + 16384, F_X2 = W_FLG + 49152, F_X = W_FLG + 65536, F_T1 = W_FLG + 131072, FLG_SZ = 262144;
constexpr u32 W_CTL = W_FLG + FLG_SZ;
constexpr u32 W_X1 = 4 * MB, W_X2 = 6 * MB, W_X3 = 8 * MB;
constexpr u32 W_OA = 10 * MB;
constexpr u32 W_PQ = 14 * MB;
constexpr u32 W_L = 16 * MB;
constexpr u32 L_KX = 0, L_VX = 16 * MB, L_QX = 32 * MB, L_OB = 48 * MB, L_KM = 64 * MB, L_VM = 68 * MB, L_SZ = 76 * MB, W_KVP = W_L + 2 * L_SZ  , W_ST = W_KVP + MB  , W_END = W_ST + 2 * MB, OOB = 0x7fffffffu;
constexpr u32 C_RING = 0, C_MEM = 524288, C_HINT = C_MEM + 32768;
DI u32 cphys(u32 o) { return W_COMM + ((o >> 13) << 14) + (o & 8191u); }
constexpr float SCALE = 0.08838834764831845f, LOG2E = 1.4426950408889634f;

struct Params { const u16 *x, *wqkv, *wo, *mem0, *Wq, *Wk, *Wv, *Wo; u16* out; char* ws; };

DI int fresh_lane() { int l; asm volatile("v_mbcnt_lo_u32_b32 %0, -1, 0\n\tv_mbcnt_hi_u32_b32 %0, -1, %0" : "=v"(l)); return l; }
#define LK(p, k) do {} while (0)
DI int fresh_lane();
DI void landed() { __builtin_amdgcn_s_waitcnt(0x0f70); }
DI void drain() { asm volatile("s_waitcnt vmcnt(0)" ::: "memory"); }
DI void post(rsrc_t rw, u32 flag, int val, int l) { if (l == 0) st4(rw, flag, (u32)val); }
template <int NAP = 2>
DI void await(rsrc_t rw, u32 off, int want, int& dead) {
  for (int sp = dead;; ++sp) {
    asm volatile("" ::: "memory");
    const int v = (int)ld4(rw, off);
    if (__builtin_amdgcn_ballot_w64(v < want) == 0) break;
    if (sp >= (1 << 19)) { dead = 1 << 19; break; }
    __builtin_amdgcn_s_sleep(NAP);
  }
  asm volatile("" ::: "memory");
}
DI void await1(rsrc_t rw, u32 off, int want, int& seen, int& dead) {
  for (int sp = dead; seen < want; ++sp) {
    asm volatile("" ::: "memory");
    seen = __builtin_amdgcn_readfirstlane((int)ld4(rw, 0, off));
    if (seen >= want) break;
    if (sp >= (1 << 19)) { dead = 1 << 19; break; }
    __builtin_amdgcn_s_sleep(2);
  }
  asm volatile("" ::: "memory");
}

DI void fill_tile(LAS char* dst, const u16* W, int ld, int row0, int nrows, int col0, int wave, int nw, int l) {
  for (int i = wave; i < nrows / 32; i += nw) {
    const u16* src = W + (size_t)(row0 + i * 32 + (l >> 1)) * ld + col0 + 8 * (l & 1);
    __builtin_amdgcn_global_load_lds((const GAS void*)src, (LAS void*)(dst + i * 1024), 16, 0, 0);
  }
}
template <bool NAT> DI u32x4 wfrag(LAS char* t, int u, int l) {
  if (NAT) { LAS char* p = t + u * 1024 + (l >> 4) * 256 + (l & 15) * 8; return cat(trd(p), trd(p + 128)); }
  LAS char* p = t + u * 1024 + l * 8; return cat(trd(p), trd(p + 512));
}
struct Stats { float mean, rstd; };
constexpr u32x4 ONES4 = {ONE2, ONE2, ONE2, ONE2};
DI void acc_stats(u32x4 x, f32x4& s1, f32x4& s2) { s1 = mfma32(ONES4, x, s1); s2 = mfma32(x, x, s2); }
DI float lane_get(float v, int srclane);
DI Stats fin_stats(f32x4 s1, f32x4 s2, int l) {
  const int n = l & 15;
  const float d = (n & 2) ? ((n & 1) ? s2[3] : s2[2]) : ((n & 1) ? s2[1] : s2[0]);
  const float q = lane_get(d, n + 16 * (n >> 2));
  const float mean = s1[0] * (1.f / 512.f), var = fmaxf(q * (1.f / 512.f) - mean * mean, 0.f);
  return {mean, __builtin_amdgcn_rsqf(var + 1e-5f)};
}
DI void put_stats(rsrc_t rw, u32 off, f32x4 s1, f32x4 s2, int l) {
  const int n = l & 15;
  const float d = (n & 2) ? ((n & 1) ? s2[3] : s2[2]) : ((n & 1) ? s2[1] : s2[0]);
  st8(rw, (l >> 4) == (n >> 2) ? off + n * 64 : OOB, (u32x2){f2u(s1[0]), f2u(d)});
}
template <int N> DI Stats sum_stats(const u32x4 (&t)[N]) {
  float a = 0.f, b = 0.f;
#pragma unroll
  for (int i = 0; i < N; ++i) { a += u2f(t[i][0]) + u2f(t[i][2]); b += u2f(t[i][1]) + u2f(t[i][3]); }
  a = xsum(a); b = xsum(b);
  const float mean = a * (1.f / 512.f), var = fmaxf(b * (1.f / 512.f) - mean * mean, 0.f);
  return {mean, __builtin_amdgcn_rsqf(var + 1e-5f)};
}
#define SGB(mask, n) __builtin_amdgcn_sched_group_barrier(mask, n, 0)
template <int NM, int ND> DI void weave() {
#pragma unroll
  for (int i = 0; i < NM; ++i) { SGB(0x8, 1); if (i < ND) SGB(0x100, 1); }
  SB();
}
DI float lane_get(float v, int srclane) { return u2f(__builtin_amdgcn_ds_bpermute(srclane * 4, (int)f2u(v))); }
DI float phi(float t) { return (t > 0.f ? t + 1.f : __expf(t)) + 1e-6f; }

template <bool NAT>
DI void load_x(u32x4 (&xf)[16], rsrc_t rx, u32 xoff, int mt, int l) {
#pragma unroll
  for (int u = 0; u < 16; ++u)
    xf[u] = NAT ? ld16p(rx, (u32)((16 * mt + (l & 15)) * 1024 + (32 * u + 8 * (l >> 4)) * 2)) : ld16(rx, xoff + mt * 16384 + u * 1024 + l * 16);
}
DI f32x4 colsum_lane(LAS char* t, int l) { f32x4 c = Z4; for (int u = 0; u < 16; ++u) c = mfma32((u32x4){ONE2, ONE2, ONE2, ONE2}, wfrag<false>(t, u, l), c); return c; }
DI f32x4 colsum_reg(LAS char* t, int l) { f32x4 c = Z4; for (int u = 0; u < 16; ++u) c = mfma32(wfrag<false>(t, u, l), (u32x4){ONE2, ONE2, ONE2, ONE2}, c); return c; }

__global__ void __launch_bounds__(256, 1) kFirst(Params p) {
  extern __shared__ __attribute__((aligned(16))) char smem[];
  LAS char* lds = (LAS char*)smem;
  const int b = blockIdx.x, tid = threadIdx.x, w = __builtin_amdgcn_readfirstlane(tid >> 6), l = tid & 63;
  if (b == 0 && w == 0) LK(p, 0);
  const int hp = b >> 4, rg = (b & 7) * 2 + ((b >> 3) & 1), s = rg * 4 + w;
#pragma unroll
  for (int m = 0; m < 3; ++m)
#pragma unroll
    for (int i = 0; i < 8; ++i)
      __builtin_amdgcn_global_load_lds((const GAS void*)(p.wqkv + (size_t)(64 * i + 16 * w + (l >> 2)) * 1536 + (m == 2 ? 0 : 512 + 512 * m) + 32 * hp + 8 * (l & 3)), (LAS void*)(lds + m * 32768 + (4 * i + w) * 1024), 16, 0, 0);
  const rsrc_t rw = mkrsrc(p.ws, W_END), rx = mkrsrc(p.x, 2 * MB);
  u32x4 x0[16], x1[16];
  const u32 xo = (32 * s + (l & 15)) * 1024 + (l >> 4) * 16;
#pragma unroll
  for (int u = 0; u < 3; ++u) { x0[u] = ld16p(rx, xo + u * 64); x1[u] = ld16p(rx, xo + u * 64, 16384); }
  if (b == 0 && tid == 0) st4(rw, W_CTL, (u32)__builtin_amdgcn_s_getreg(20 | (3 << 11)) & 7u);
  landed(); __syncthreads();
  const u32x4 SENT = {0xffffffffu, 0xffffffffu, 0xffffffffu, 0xffffffffu};
  st16(rw, b * 4096 + tid * 16, SENT); st16(rw, b * 4096 + tid * 16, SENT, MB); st16(rw, w == 0 ? b * 1024 + l * 16 : OOB, SENT, 2 * MB);
  f32x4 acc[6][2], s1a = Z4, s2a = Z4, s1b = Z4, s2b = Z4, c0 = Z4, c1 = Z4;
  u32x4 wf[2][8];
  LAS char* p0 = lds + (l >> 4) * 512 + ((l & 15) >> 2) * 64 + (l & 3) * 8; LAS char* p1 = p0 + 65536; LAS char* p2 = p0 + (w >> 1) * 32768 + (w & 1) * 32; LAS char* p3 = p1 + (w & 1) * 32;
  asm volatile("" : "+v"(p0), "+v"(p1), "+v"(p2), "+v"(p3));
  auto wfr = [&](int T, int u) { LAS char* q = (T < 4 ? p0 + (T >> 1) * 32768 + (T & 1) * 32 : (T < 6 ? p1 + (T & 1) * 32 : (T == 6 ? p2 : p3))) + u * 2048; return cat(trd(q), trd(q + 256)); };
#pragma unroll
  for (int T = 0; T < 8; ++T) wf[0][T] = wfr(T, 0);
#pragma unroll
  for (int T = 0; T < 6; ++T) acc[T][0] = acc[T][1] = Z4;
  SB();
#pragma unroll
  for (int u = 0; u < 16; ++u) {
#pragma unroll
    for (int T = 0; T < 8; ++T) if (u < 15) wf[(u + 1) & 1][T] = wfr(T, u + 1);
    if (u + 3 < 16) { x0[u + 3] = ld16p(rx, xo + (u + 3) * 64); x1[u + 3] = ld16p(rx, xo + (u + 3) * 64, 16384); }
    acc_stats(x0[u], s1a, s2a); acc_stats(x1[u], s1b, s2b);
    c0 = mfma32(ONES4, wf[u & 1][6], c0); c1 = mfma32(wf[u & 1][7], ONES4, c1);
#pragma unroll
    for (int T = 0; T < 6; ++T) {
      acc[T][0] = T < 4 ? mfma32(x0[u], wf[u & 1][T], acc[T][0]) : mfma32(wf[u & 1][T], x0[u], acc[T][0]);
      acc[T][1] = T < 4 ? mfma32(x1[u], wf[u & 1][T], acc[T][1]) : mfma32(wf[u & 1][T], x1[u], acc[T][1]);
    }
    SGB(0x20, 1);
#pragma unroll
    for (int i = 0; i < 18; ++i) { SGB(0x8, 1); if (i < 16) SGB(0x100, 1); if (i == 8) SGB(0x20, 1); }
    SB();
  }
  LAS char* ex = lds + 98304;
  *(LAS float*)(ex + w * 256 + l * 4) = c0[0];
  if (w < 2) *(LAS f32x4*)(ex + 1024 + w * 1024 + l * 16) = c1;
  const Stats sa = fin_stats(s1a, s2a, l), sb = fin_stats(s1b, s2b, l);
  f32x4 ma, ra, mb, rb;
#pragma unroll
  for (int r = 0; r < 4; ++r) { const int sl = 4 * (l >> 4) + r; ma[r] = lane_get(sa.mean, sl); ra[r] = lane_get(sa.rstd, sl); mb[r] = lane_get(sb.mean, sl); rb[r] = lane_get(sb.rstd, sl); }
  __syncthreads();
#pragma unroll
  for (int hh = 0; hh < 2; ++hh) {
    const float csk = *(LAS float*)(ex + hh * 256 + l * 4), csv = *(LAS float*)(ex + (2 + hh) * 256 + l * 4);
    const f32x4 csq = *(LAS f32x4*)(ex + 1024 + hh * 1024 + l * 16);
    f32x4 ka = ra * (acc[hh][0] - ma * csk), kb = rb * (acc[hh][1] - mb * csk), qa = sa.rstd * (acc[4 + hh][0] - sa.mean * csq), qb = sb.rstd * (acc[4 + hh][1] - sb.mean * csq);
#pragma unroll
    for (int r = 0; r < 4; ++r) { ka[r] = phi(ka[r]); kb[r] = phi(kb[r]); qa[r] = phi(qa[r]); qb[r] = phi(qb[r]); }
    const u32x4 pk = cat(pk4(ka), pk4(kb)), vv = cat(pk4(ra * (acc[2 + hh][0] - ma * csv)), pk4(rb * (acc[2 + hh][1] - mb * csv)));
    *(LAS f32x4*)(ex + 4096 + (hh * 4 + w) * 2048 + l * 16) = mfma32(pk, vv, Z4);
    *(LAS f32x4*)(ex + 4096 + (hh * 4 + w) * 2048 + 1024 + l * 16) = mfma32(pk, ONES4, Z4);
    st16(rw, W_PQ + ((2 * hp + hh) * 64 + s) * 1024 + l * 16, cat(pk4(qa), pk4(qb)));
  }
  __syncthreads();
  LAS char* q = ex + 4096 + (w >> 1) * 8192 + (w & 1) * 1024 + l * 16;
  const f32x4 t = (*(LAS f32x4*)q + *(LAS f32x4*)(q + 2048)) + (*(LAS f32x4*)(q + 4096) + *(LAS f32x4*)(q + 6144));
  st16(rw, W_KVP + ((2 * hp + (w >> 1)) * 16 + rg) * 2048 + (w & 1) * 1024 + l * 16, BC(u32x4, t));
}

constexpr int A_WQ = 0, A_WO = 49152, A_PQ = 65536, A_RED = 131072;
DI u32x2 addres(u32x2 res, f32x4 acc) {
  const u32x2 br = pk4(acc);
  return pk4((f32x4){bflo(res[0]) + bflo(br[0]), bfhi(res[0]) + bfhi(br[0]), bflo(res[1]) + bflo(br[1]), bfhi(res[1]) + bfhi(br[1])});
}
template <int LAYER>
DI void A_out(const Params& p, LAS char* lds, f32x4 kv, f32x4 z, u32x4 pq0, int a, int w, int l, int& dead) {
  const rsrc_t rw = mkrsrc(p.ws, W_END), rx = mkrsrc(p.x, 2 * MB);
  const u32 oa = W_OA + LAYER * 2 * MB, xout = LAYER ? W_X3 : W_X1, fo = F_OA + LAYER * 4096, fx = F_X + LAYER * 16384 + a * 128;
  const u32x2 kvA = pk4(kv), zA = pk4(z);
  auto o_seg = [&](int s, u32x4 pq) {
    u32x2 o[2];
#pragma unroll
    for (int i = 0; i < 2; ++i) {
      const u32x2 q = {pq[2 * i], pq[2 * i + 1]};
      const f32x4 num = mfma16(kvA, q, Z4), den = mfma16(zA, q, Z4);
      o[i] = pk4(num * __builtin_amdgcn_rcpf(den[0] + 1e-6f));
    }
    st16(rw, oa + (s * 32 + a) * 1024 + l * 16, cat(o[0], o[1]));
  };
  auto resid = [&](int s, u32x2 (&r)[2]) {
    const u32 xo = s * 32768 + (a >> 1) * 1024 + l * 16 + (a & 1) * 8, no = (32 * s + (l & 15)) * 1024 + (16 * a + 4 * (l >> 4)) * 2;
    r[0] = LAYER ? ld8(rw, W_X2 + xo) : ld8p(rx, no); r[1] = LAYER ? ld8(rw, W_X2 + xo + 16384) : ld8p(rx, no + 16384);
  };
  auto finish = [&](int s, const u32x2 (&r)[2], f32x4 a0, f32x4 a1) {
    const u32 xo = s * 32768 + (a >> 1) * 1024 + l * 16 + (a & 1) * 8;
    const u32x2 y0 = addres(r[0], a0), y1 = addres(r[1], a1), one = {ONE2, ONE2};
    st8(rw, xout + xo, y0); st8(rw, xout + xo + 16384, y1);
    const u32 so = W_ST + LAYER * 524288 + s * 8192 + ((a >> 1) & 3) * 1024 + (a >> 3) * 16 + (a & 1) * 8;
    put_stats(rw, so, mfma16(one, y0, Z4), mfma16(y0, y0, Z4), l); put_stats(rw, so + 4096, mfma16(one, y1, Z4), mfma16(y1, y1, Z4), l);
  };
  o_seg(w, pq0);
  u32x2 rs[2]; resid(w, rs);
  drain(); bar();
  if (w == 0) post(rw, fo + a * 128, 0, l);
  if (LAYER == 0) {
    for (int i = w; i < 64; i += 4) __builtin_amdgcn_global_load_lds((const GAS void*)(p.ws + W_PQ + a * 65536 + i * 1024 + l * 16), (LAS void*)(lds + A_PQ + i * 1024), 16, 0, 0);
    drain();
  }
#pragma unroll
  for (int i = 1; i < 8; ++i) o_seg(4 * i + w, *(LAS u32x4*)(lds + A_PQ + (4 * i + w) * 1024 + l * 16));
  await(rw, fo + (l & 31) * 128, 0, dead);
  {
    u32x4 o[4][8]; u32x2 wf[8];
#pragma unroll
    for (int u = 0; u < 8; ++u) o[0][u] = ld16(rw, oa + (8 * w + u) * 1024 + l * 16);
#pragma unroll
    for (int u = 0; u < 8; ++u) wf[u] = trd(lds + A_WO + (8 * w + u) * 512 + l * 8);
    SB();
#pragma unroll
    for (int s = 0; s < 4; ++s) {
      f32x4 a0 = Z4, a1 = Z4;
#pragma unroll
      for (int u = 0; u < 8; ++u) {
        a0 = mfma16(wf[u], (u32x2){o[s][u][0], o[s][u][1]}, a0); a1 = mfma16(wf[u], (u32x2){o[s][u][2], o[s][u][3]}, a1);
      }
      LAS char* red = lds + A_RED + s * 8192 + l * 16;
      *(LAS f32x4*)(red + w * 2048) = a0; *(LAS f32x4*)(red + w * 2048 + 1024) = a1;
      bar();
      if (s == 0 && w == 0) post(rw, fo + a * 128, 1, l);
      if (w == s) {
        a0 = (*(LAS f32x4*)red + *(LAS f32x4*)(red + 2048)) + (*(LAS f32x4*)(red + 4096) + *(LAS f32x4*)(red + 6144));
        a1 = (*(LAS f32x4*)(red + 1024) + *(LAS f32x4*)(red + 3072)) + (*(LAS f32x4*)(red + 5120) + *(LAS f32x4*)(red + 7168));
        finish(s, rs, a0, a1);
      }
      SB();
      if (s == 0) {
#pragma unroll
        for (int t = 1; t < 4; ++t)
#pragma unroll
          for (int u = 0; u < 8; ++u) o[t][u] = ld16(rw, oa + (t * 32 + 8 * w + u) * 1024 + l * 16);
        SB();
        if (w == 0) { asm volatile("s_waitcnt vmcnt(24)" ::: "memory"); post(rw, fx, 0, l); }
      }
    }
    drain(); post(rw, fx + w * 4096, 0, l);
  }
  u32x2 wf[32];
#pragma unroll
  for (int u = 0; u < 32; ++u) wf[u] = trd(lds + A_WO + u * 512 + l * 8);
  await(rw, fo + (l & 31) * 128, 1, dead);
  u32x4 o[32];
#pragma unroll
  for (int u = 0; u < 32; ++u) o[u] = ld16(rw, oa + ((w + 4) * 32 + u) * 1024 + l * 16);
  landed();
#pragma unroll 1
  for (int s = w + 4; s < 64; s += 4) {
    if (s >= 8 && s < 12) {
#pragma unroll 4
      for (int i = 8; i < 16; ++i) o_seg(4 * i + w, *(LAS u32x4*)(lds + A_PQ + (4 * i + w) * 1024 + l * 16));
      drain(); bar();
      if (w == 0) post(rw, fo + a * 128, 2, l);
    }
    if (s + 4 >= 32 && s < 32) await(rw, fo + (l & 31) * 128, 2, dead);
    resid(s, rs);
    u32 nxt = s < 60 ? oa + (s + 4) * 32768 + l * 16 : OOB; asm volatile("" : "+v"(nxt));
    f32x4 a0 = Z4, a1 = Z4;
#pragma unroll
    for (int u = 0; u < 32; ++u) {
      a0 = mfma16(wf[u], (u32x2){o[u][0], o[u][1]}, a0); a1 = mfma16(wf[u], (u32x2){o[u][2], o[u][3]}, a1);
      o[u] = ld16(rw, nxt + (u & 3) * 1024, (u >> 2) * 4096);
      SB();
    }
    finish(s, rs, a0, a1);
    drain(); post(rw, fx + w * 4096, s >> 2, l);
  }
}
DI void a_role(const Params& p, LAS char* lds, int a, int w) {
  const int l = fresh_lane();
  const rsrc_t rw = mkrsrc(p.ws, W_END);
  int dead = 0;
  f32x4 kv = Z4, z = Z4;
  u32x4 part[32];
#pragma unroll
  for (int i = 0; i < 32; ++i) part[i] = ld16p(rw, W_KVP + a * 32768 + i * 1024 + l * 16);
  const u32x4 pq0 = ld16p(rw, W_PQ + (a * 64 + w) * 1024 + l * 16);
  SB();
  fill_tile(lds + A_WO, p.wo, 512, 0, 512, 16 * a, w, 4, l);
#pragma unroll
  for (int i = 0; i < 16; ++i) { kv += BC(f32x4, part[2 * i]); z += BC(f32x4, part[2 * i + 1]); }
  A_out<0>(p, lds, kv, z, pq0, a, w, l, dead);
  for (int m = 0; m < 3; ++m) fill_tile(lds + A_WQ + m * 16384, p.wqkv, 1536, 0, 512, m * 512 + 16 * a, w, 4, l);
  drain(); __syncthreads();
  kv = Z4; z = Z4;
  {
    u32x4 wf[3][16];
    f32x4 csq = Z4, ck = Z4, cv = Z4;
#pragma unroll
    for (int u = 0; u < 16; ++u) {
#pragma unroll
      for (int m = 0; m < 3; ++m) wf[m][u] = wfrag<false>(lds + A_WQ + m * 16384, u, l);
      csq = mfma32(wf[0][u], ONES4, csq); ck = mfma32(ONES4, wf[1][u], ck); cv = mfma32(ONES4, wf[2][u], cv);
    }
    const float csk = ck[0], csv = cv[0];
    const u32 fo = F_X2 + ((w & 1) * 16 + (l & 15)) * 128, so = W_ST + MB + (w & 1) * 2048 + (l & 15) * 64 + (l >> 4) * 16;
#pragma unroll 1
    for (int s = w >> 1; s < 64; s += 2) {
      if (s < 62) await<12>(rw, fo + (s & 3) * 4096, s >> 2, dead); else await<1>(rw, fo + (s & 3) * 4096, s >> 2, dead);
      u32x4 xf[16]; load_x<false>(xf, rw, W_X2, 2 * s + (w & 1), l);
      const u32x4 sp[2] = {ld16(rw, so + s * 4096), ld16(rw, so + s * 4096 + 1024)};
      SB();
      f32x4 aq = Z4, ak = Z4, av = Z4;
#pragma unroll
      for (int u = 0; u < 16; ++u) { ak = mfma32(xf[u], wf[1][u], ak); av = mfma32(xf[u], wf[2][u], av); aq = mfma32(wf[0][u], xf[u], aq); }
      const Stats st = sum_stats(sp);
      f32x4 pk, vv, pq;
#pragma unroll
      for (int r = 0; r < 4; ++r) {
        const float mean = lane_get(st.mean, 4 * (l >> 4) + r), rstd = lane_get(st.rstd, 4 * (l >> 4) + r);
        pk[r] = phi(rstd * (ak[r] - mean * csk)); vv[r] = rstd * (av[r] - mean * csv); pq[r] = phi(st.rstd * (aq[r] - st.mean * csq[r]));
      }
      const u32x2 pa = pk4(pk);
      kv = mfma16(pa, pk4(vv), kv); z = mfma16(pa, (u32x2){ONE2, ONE2}, z);
      *(LAS u32x2*)(lds + A_PQ + s * 1024 + l * 16 + (w & 1) * 8) = pk4(pq);
    }
  }
  *(LAS f32x4*)(lds + A_RED + w * 2048 + l * 16) = kv; *(LAS f32x4*)(lds + A_RED + w * 2048 + 1024 + l * 16) = z;
  bar();
  LAS char* red = lds + A_RED + l * 16;
  kv = (*(LAS f32x4*)red + *(LAS f32x4*)(red + 2048)) + (*(LAS f32x4*)(red + 4096) + *(LAS f32x4*)(red + 6144));
  z = (*(LAS f32x4*)(red + 1024) + *(LAS f32x4*)(red + 3072)) + (*(LAS f32x4*)(red + 5120) + *(LAS f32x4*)(red + 7168));
  A_out<1>(p, lds, kv, z, *(LAS u32x4*)(lds + A_PQ + w * 1024 + l * 16), a, w, l, dead);
}

constexpr int B_STG = 98304, B_RED = B_STG + 32768, B_CS = B_RED + 12288, B_SEEN = B_CS + 6144;
template <bool ISV>
DI void T1_unit(LAS char* t, u32x4 (&x0)[16], u32x4 (&x1)[16], rsrc_t rw, u32 so, u32 nxt, u32 fx, u32 ft, u32 lb, int s, int h, int qd, int l, int& dead
) {
  const int seen = (int)ld4(rw, fx);
  u32x4 pa[4], pb[4];
#pragma unroll
  for (int i = 0; i < 4; ++i) { pa[i] = ld16(rw, so + i * 1024); pb[i] = ld16(rw, so + 4096 + i * 1024); }
  f32x4 acc[6][2];
  u32x4 wf[2][6];
  LAS char* p0 = t + l * 8; LAS char* p1 = p0 + 65536;
  asm volatile("" : "+v"(p0), "+v"(p1), "+v"(nxt));
  auto wfr = [&](int T, int u) { LAS char* q = (T < 4 ? p0 : p1) + (T & 3) * 16384 + u * 1024; return cat(trd(q), trd(q + 512)); };
#pragma unroll
  for (int T = 0; T < 6; ++T) { acc[T][0] = acc[T][1] = Z4; wf[0][T] = wfr(T, 0); }
  SB();
#pragma unroll
  for (int u = 0; u < 16; ++u) {
#pragma unroll
    for (int T = 0; T < 6; ++T) if (u < 15) wf[(u + 1) & 1][T] = wfr(T, u + 1);
#pragma unroll
    for (int T = 0; T < 6; ++T) {
      acc[T][0] = (ISV && T < 4) ? mfma32(x0[u], wf[u & 1][T], acc[T][0]) : mfma32(wf[u & 1][T], x0[u], acc[T][0]);
      acc[T][1] = (ISV && T < 4) ? mfma32(x1[u], wf[u & 1][T], acc[T][1]) : mfma32(wf[u & 1][T], x1[u], acc[T][1]);
    }
#pragma unroll
    for (int i = 0; i < 3; ++i) {
      const int k = 3 * (u - 6) + i, v = k >> 1;
      if (u >= 6 && v < u) (k & 1 ? x1 : x0)[v] = ld16(rw, nxt + (v & 3) * 1024, (v >> 2) * 4096 + (k & 1) * 16384);
    }
#pragma unroll
    for (int i = 0; i < 12; ++i) { SGB(0x8, 1); SGB(0x100, 1); if ((i & 3) == 3) SGB(0x20, 1); }
    SB();
    if (u == 5) {
      if (__builtin_amdgcn_ballot_w64(seen < (min(s + 4, 63) >> 2))) await(rw, fx, min(s + 4, 63) >> 2, dead);
      post(rw, ft, (s >> 2) - 1, l);
    }
  }
  x0[15] = ld16(rw, nxt + 3072, 12288); x1[15] = ld16(rw, nxt + 3072, 12288 + 16384);
  const Stats sa = sum_stats(pa), sb = sum_stats(pb);
  f32x4 cs[6];
#pragma unroll
  for (int T = 0; T < 6; ++T) cs[T] = *(LAS f32x4*)(t + B_CS + T * 1024 + l * 16);
  const u32 base = lb + (s * 32 + h) * 8192 + l * 16; const int hf = qd & 1;
  if (ISV) {
    f32x4 ma, ra, mb, rb;
#pragma unroll
    for (int r = 0; r < 4; ++r) { const int sl = 4 * (l >> 4) + r; ma[r] = lane_get(sa.mean, sl); ra[r] = lane_get(sa.rstd, sl); mb[r] = lane_get(sb.mean, sl); rb[r] = lane_get(sb.rstd, sl); }
#pragma unroll
    for (int T = 0; T < 4; ++T) st16(rw, base + L_VX + (4 * hf + T) * 1024, cat(pk4(ra * (acc[T][0] - ma * cs[T][0])), pk4(rb * (acc[T][1] - mb * cs[T][0]))));
  } else {
    const float ka = sa.rstd * (SCALE * LOG2E), kb = sb.rstd * (SCALE * LOG2E);
#pragma unroll
    for (int T = 0; T < 4; T += 2) {
      st16(rw, base + L_KX + (2 * hf + (T >> 1)) * 1024, cat(pk4(ka * (acc[T][0] - sa.mean * cs[T])), pk4(ka * (acc[T + 1][0] - sa.mean * cs[T + 1]))));
      st16(rw, base + L_KX + 4096 + (2 * hf + (T >> 1)) * 1024, cat(pk4(kb * (acc[T][1] - sb.mean * cs[T])), pk4(kb * (acc[T + 1][1] - sb.mean * cs[T + 1]))));
    }
  }
  st16(rw, base + L_QX + qd * 1024, cat(pk4(sa.rstd * (acc[4][0] - sa.mean * cs[4])), pk4(sa.rstd * (acc[5][0] - sa.mean * cs[5]))));
  st16(rw, base + L_QX + 4096 + qd * 1024, cat(pk4(sb.rstd * (acc[4][1] - sb.mean * cs[4])), pk4(sb.rstd * (acc[5][1] - sb.mean * cs[5]))));
}
template <bool ISV, int NT>
DI bool T1_part(LAS char* t, u32x4 (&x)[16], rsrc_t rw, u32 so, u32 nxt, u32 fn, int want, u32 lb, int s, int h, int qd, int w, int l) {
  const int mt = w & 1, th = NT == 3 ? w >> 1 : 0;
  const int seen = (int)ld4(rw, fn);
  u32x4 sp[4];
#pragma unroll
  for (int i = 0; i < 4; ++i) sp[i] = ld16(rw, so + s * 8192 + mt * 4096 + i * 1024);
  constexpr int NK = NT * 2 / 3;
  f32x4 acc[NT];
  u32x4 wf[2][NT];
  LAS char* p0 = t + l * 8 + th * 32768; LAS char* p1 = t + l * 8 + 65536 + th * 16384;
  asm volatile("" : "+v"(p0), "+v"(p1));
  auto wfr = [&](int T, int u) { LAS char* q = (T < NK ? p0 + T * 16384 : p1 + (T - NK) * 16384) + u * 1024; return cat(trd(q), trd(q + 512)); };
#pragma unroll
  for (int T = 0; T < NT; ++T) { acc[T] = Z4; wf[0][T] = wfr(T, 0); }
  SB();
  bool got = false;
#pragma unroll
  for (int u = 0; u < 16; ++u) {
#pragma unroll
    for (int T = 0; T < NT; ++T) if (u < 15) wf[(u + 1) & 1][T] = wfr(T, u + 1);
#pragma unroll
    for (int T = 0; T < NT; ++T) acc[T] = (ISV && T < NK) ? mfma32(x[u], wf[u & 1][T], acc[T]) : mfma32(wf[u & 1][T], x[u], acc[T]);
#pragma unroll
    for (int T = 0; T < NT; ++T) { SGB(0x8, 1); SGB(0x100, 2); }
    SB();
    if (u == 7) { got = __builtin_amdgcn_ballot_w64(seen < want) == 0; nxt = got ? nxt : OOB; }
    if (u >= 8) {
#pragma unroll
      for (int v = 2 * (u - 8); v < 2 * (u - 7); ++v) x[v] = ld16(rw, nxt + (v & 3) * 1024, (v >> 2) * 4096);
      SB();
    }
  }
  const Stats st = sum_stats(sp);
  f32x4 cs[NT];
#pragma unroll
  for (int T = 0; T < NT; ++T) cs[T] = *(LAS f32x4*)(t + B_CS + (T < NK ? 2 * th + T : 4 + th + T - NK) * 1024 + l * 16);
  const u32 base = lb + (s * 32 + h) * 8192 + l * 16; const int hf = qd & 1;
  if (ISV) {
    f32x4 m, r;
#pragma unroll
    for (int i = 0; i < 4; ++i) { m[i] = lane_get(st.mean, 4 * (l >> 4) + i); r[i] = lane_get(st.rstd, 4 * (l >> 4) + i); }
#pragma unroll
    for (int T = 0; T < NK; ++T) st8(rw, base + L_VX + (4 * hf + 2 * th + T) * 1024 + mt * 8, pk4(r * (acc[T] - m * cs[T][0])));
  } else {
    const float k = st.rstd * (SCALE * LOG2E);
#pragma unroll
    for (int T = 0; T < NK; T += 2) st16(rw, base + L_KX + mt * 4096 + (2 * hf + th + (T >> 1)) * 1024, cat(pk4(k * (acc[T] - st.mean * cs[T])), pk4(k * (acc[T + 1] - st.mean * cs[T + 1]))));
  }
  if (NT == 3) st8(rw, base + L_QX + mt * 4096 + qd * 1024 + th * 8, pk4(st.rstd * (acc[NK] - st.mean * cs[NK])));
  else st16(rw, base + L_QX + mt * 4096 + qd * 1024, cat(pk4(st.rstd * (acc[NK] - st.mean * cs[NK])), pk4(st.rstd * (acc[NT - 1] - st.mean * cs[NT - 1]))));
  return got;
}
template <bool ISV>
DI void T1_all(const Params& p, LAS char* lds, int layer, int j, int w, int l, int& dead) {
  const rsrc_t rw = mkrsrc(p.ws, W_END);
  const u32 xin = layer ? W_X3 : W_X1, lb = W_L + layer * L_SZ, fx = F_X + layer * 16384 + (l & 31) * 128, ft = F_T1 + layer * 65536 + (j * 4 + w) * 128;
  const u32 so = W_ST + layer * 524288 + (l & 15) * 64 + (l >> 4) * 16;
  const int s0 = w == 3 ? 7 : w + 8;
  u32x4 x0[16], x1[16];
  await<1>(rw, fx, 0, dead);
  load_x<false>(x0, rw, xin, w & 1, l);
#pragma unroll 1
  for (int r = 0; r < 4; ++r) {
    const int sn = r < 3 ? 2 * r + 1 + (w >> 1) : s0;
    const u32 nxt = xin + l * 16 + sn * 32768 + (r < 3 ? (w & 1) * 16384 : 0), fn = fx + (sn & 3) * 4096;
    const bool got = r == 0 ? T1_part<ISV, 3>(lds, x0, rw, so, nxt, fn, sn >> 2, lb, 0, j >> 2, j & 3, w, l)
                            : T1_part<ISV, 6>(lds, x0, rw, so, nxt, fn, sn >> 2, lb, 2 * r - 1 + (w >> 1), j >> 2, j & 3, w, l);
    const int again = (int)ld4(rw, fn);
    drain(); bar();
    post(rw, ft, ((2 * r + 4 - w) >> 2) - 1, l);
    if (r == 0) { if (j == 96 && w == 0) LK(p, 2 + 2 * layer); }
    if (!got) {
      if (__builtin_amdgcn_ballot_w64(again < (sn >> 2))) await<1>(rw, fn, sn >> 2, dead);
#pragma unroll
      for (int u = 0; u < 16; ++u) x0[u] = ld16(rw, nxt + (u & 3) * 1024, (u >> 2) * 4096);
    }
  }
  load_x<false>(x1, rw, xin, 2 * s0 + 1, l);
  landed();
#pragma unroll 1
  for (int s = s0; s < 64; s += 4) {
    T1_unit<ISV>(lds, x0, x1, rw, so + s * 8192, s < 60 ? xin + (s + 4) * 32768 + l * 16 : OOB, fx + w * 4096, ft, lb, s, j >> 2, j & 3, l, dead
);
  }
  drain(); post(rw, ft, 15, l);
}
DI void T3_unit(rsrc_t rw, u32 lb, u32 fkvm, int want, int& seen, int s, int h, int w, int l, int& dead) {
  const int qt = w & 1, fh = w >> 1, g = l >> 4, n = l & 15;
  const u32 sh = (s * 32 + h);
  u32x4 q[4], k0[4], k1[4], km[4], v[4], vm[2];
#pragma unroll
  for (int u = 0; u < 4; ++u) {
    q[u] = ld16(rw, lb + L_QX + sh * 8192 + qt * 4096 + u * 1024 + l * 16);
    k0[u] = ld16(rw, lb + L_KX + sh * 8192 + u * 1024 + l * 16);
    k1[u] = ld16(rw, qt ? lb + L_KX + sh * 8192 + 4096 + u * 1024 + l * 16 : OOB);
    v[u] = ld16(rw, lb + L_VX + sh * 8192 + (4 * fh + u) * 1024 + l * 16);
  }
  SB();
  await1(rw, fkvm, want, seen, dead);
#pragma unroll
  for (int u = 0; u < 4; ++u) km[u] = ld16(rw, lb + L_KM + sh * 2048 + u * 512 + g * 128 + (2 * (n >> 2) + (n & 1)) * 16);
  vm[0] = ld16(rw, lb + L_VM + sh * 4096 + (2 * fh) * 1024 + l * 16); vm[1] = ld16(rw, lb + L_VM + sh * 4096 + (2 * fh + 1) * 1024 + l * 16);
  SB();
  f32x4 s0 = Z4, s1 = Z4, sm = Z4;
#pragma unroll
  for (int u = 0; u < 4; ++u) { s0 = mfma32(k0[u], q[u], s0); s1 = mfma32(k1[u], q[u], s1); sm = mfma32(km[u], q[u], sm); }
  const int qi = 16 * qt + n;
  float mx = -3.0e38f;
#pragma unroll
  for (int r = 0; r < 4; ++r) {
    s0[r] = (4 * g + r <= qi) ? s0[r] : -1e9f * LOG2E; s1[r] = (16 + 4 * g + r <= qi) ? s1[r] : -1e9f * LOG2E; sm[r] *= SCALE * LOG2E;
    mx = fmaxf(mx, fmaxf(fmaxf(s0[r], s1[r]), sm[r]));
  }
  mx = xmax(mx);
#pragma unroll
  for (int r = 0; r < 4; ++r) { s0[r] = __builtin_amdgcn_exp2f(s0[r] - mx); s1[r] = __builtin_amdgcn_exp2f(s1[r] - mx); sm[r] = __builtin_amdgcn_exp2f(sm[r] - mx); }
  u32 zero = 0u; asm volatile("" : "+v"(zero));
  const u32x4 px = cat(pk4(s0), pk4(s1)), pm = cat(pk4(sm), (u32x2){zero, zero});
  u32 one = ONE2, half = HALF2; asm volatile("" : "+v"(one), "+v"(half));
  f32x4 ls = mfma32((u32x4){one, one, one, one}, px, Z4); ls = mfma32((u32x4){half, half, half, half}, pm, ls);
  const float inv = __builtin_amdgcn_rcpf(ls[0]);
#pragma unroll
  for (int i = 0; i < 2; ++i) {
    f32x4 oa = mfma32(v[2 * i], px, Z4), ob = mfma32(v[2 * i + 1], px, Z4);
    oa = mfma32((u32x4){vm[i][0], vm[i][1], zero, zero}, pm, oa); ob = mfma32((u32x4){vm[i][2], vm[i][3], zero, zero}, pm, ob);
    st16(rw, lb + L_OB + ((2 * s + qt) * 32 + h) * 4096 + (2 * fh + i) * 1024 + l * 16, cat(pk4(oa * inv), pk4(ob * inv)));
  }
}

DI void T4_load_w(u32x4 (&wo)[64], LAS char* stage, const u16* Wo, int bq, int w, int l) {
  const rsrc_t r = mkrsrc(Wo + 1024 * w * 512 + 32 * bq, MB - 64 * bq);
#pragma unroll
  for (int i = 0; i < 48; ++i) wo[i] = ld16p(r, (l >> 2) * 1024 + (l & 3) * 16, i * 16384);
  SB();
#pragma unroll
  for (int rd = 0; rd < 8; ++rd) {
#pragma unroll
    for (int i = 48 + 8 * rd; i < 56 + 8 * rd; ++i) if (i < 64) wo[i] = ld16p(r, (l >> 2) * 1024 + (l & 3) * 16, i * 16384);
#pragma unroll
    for (int i = 0; i < 8; ++i) *(LAS u32x4*)(stage + w * 8192 + ((l >> 1) & 1) * 4096 + (16 * i + (l >> 2)) * 32 + (l & 1) * 16) = wo[rd * 8 + i];
#pragma unroll
    for (int u = 0; u < 8; ++u) wo[rd * 8 + u] = wfrag<false>(stage + w * 8192 + (u & 1) * 4096, u >> 1, l);
#pragma unroll
    for (int u = 0; u < 8; ++u) asm volatile("" : "+a"(wo[rd * 8 + u]) :: "memory");
  }
}
DI void T4_fetch(u32x4 (&of)[33], rsrc_t rw, bool LAST, int mt, int bq, int w, int l) {
  const u32 o = W_L + (LAST ? L_SZ : 0) + L_OB + mt * 131072 + w * 32768 + l * 16;
#pragma unroll
  for (int u = 0; u < 32; ++u) of[u] = ld16(rw, o + (u & 3) * 1024, (u >> 2) * 4096);
  of[32] = ld16(rw, (LAST ? W_X3 : W_X1) + mt * 16384 + bq * 1024 + l * 16);
  SB();
}
DI void T4_unit(const u32x4 (&wo)[64], const u32x4 (&of)[33], LAS char* red, rsrc_t rw, rsrc_t ro, bool LAST, int mt, int bq, int w, int l) {
  const u32 xo = mt * 16384 + bq * 1024 + l * 16;
  const u32x4 res = of[32];
  f32x4 a0 = Z4, a1 = Z4;
#pragma unroll
  for (int u = 0; u < 32; ++u) { a0 = mfma32(wo[2 * u], of[u], a0); a1 = mfma32(wo[2 * u + 1], of[u], a1); }
  if (w) { *(LAS f32x4*)(red + (w - 1) * 2048 + l * 16) = a0; *(LAS f32x4*)(red + (w - 1) * 2048 + 1024 + l * 16) = a1; }
  bar();
  if (w == 0) {
#pragma unroll
    for (int i = 0; i < 3; ++i) { a0 += *(LAS f32x4*)(red + i * 2048 + l * 16); a1 += *(LAS f32x4*)(red + i * 2048 + 1024 + l * 16); }
    const u32x2 y0 = addres((u32x2){res[0], res[1]}, a0), y1 = addres((u32x2){res[2], res[3]}, a1);
    if (LAST) {
      const auto e0 = __builtin_amdgcn_permlane16_swap(y0[0], y1[0], false, false), e1 = __builtin_amdgcn_permlane16_swap(y0[1], y1[1], false, false);
      const int g = l >> 4;
      __builtin_amdgcn_raw_buffer_store_b128((u32x4){e0[0], e1[0], e0[1], e1[1]}, ro, (16 * mt + (l & 15)) * 1024 + (32 * bq + 16 * (g & 1) + 8 * (g >> 1)) * 2, 0, OUTPOL);
    } else {
      const u32x4 y = cat(y0, y1);
      st16(rw, W_X2 + xo, y);
      put_stats(rw, W_ST + MB + mt * 2048 + ((bq >> 1) & 1) * 1024 + (bq >> 2) * 16 + (bq & 1) * 8, mfma32(ONES4, y, Z4), mfma32(y, y, Z4), l);
    }
  }
}
DI void B_rows(const Params& p, LAS char* lds, int LAYER, int j, int w, int l, int& dead) {
  const rsrc_t rw = mkrsrc(p.ws, W_END), ro = mkrsrc(p.out, 2 * MB);
  l = fresh_lane();
  if (j & 2) T1_all<true>(p, lds, LAYER, j, w, l, dead); else T1_all<false>(p, lds, LAYER, j, w, l, dead);
  l = fresh_lane(); asm volatile("" : "+s"(j), "+s"(w));
  const int a = j & 31, c = j >> 5;
  u32x4 wo[64]; T4_load_w(wo, lds + B_STG, p.Wo, j & 15, w, l);
  await(rw, F_T1 + LAYER * 65536 + ((a * 4 + (l & 3)) * 4 + c) * 128, 15, dead);
  int seen = -1, n3 = c, n4 = c;
  const u32 fob = F_OB + LAYER * 16384 + (c * 32 + (l & 31)) * 128;
  bool ahead = false;
  auto posted = [&]() {
    bar();
    if (w == 0) { post(rw, F_OB + LAYER * 16384 + j * 128, n3 >> 2, l); if (LAYER == 0) post(rw, F_X2 + j * 128, (n4 >> 2) - 1, l); }
    if (n3 == 63 && j == 96 && w == 0) LK(p, 3 + 2 * LAYER);
    n3 += 4;
    ahead = n3 < 64 && __builtin_amdgcn_readfirstlane(*(LAS int*)(lds + B_SEEN)) > LAYER * 64 + n3;
  };
  auto project = [&](const u32x4 (&of)[33]) {
    T4_unit(wo, of, lds + B_RED + (n4 & 4) * 1536, rw, ro, LAYER == 1, 2 * n4 + (a >> 4), a & 15, w, l);
    if (LAYER == 0 && w == 0 && !ahead) { drain(); post(rw, F_X2 + j * 128, n4 >> 2, l); }
    n4 += 4;
  };
#pragma unroll 1
  while (n4 < 64) {
    if (n3 < 64 && (n3 == n4 || ahead)) {
      const int obs = (int)ld4(rw, fob), kvs = (int)ld4(rw, 0, F_KVM + a * 128);
      T3_unit(rw, W_L + LAYER * L_SZ, F_KVM + a * 128, LAYER * 64 + n3 + 1, seen, n3, a, w, l, dead);
      seen = max(seen, __builtin_amdgcn_readfirstlane(kvs));
      if (w == 0) *(LAS int*)(lds + B_SEEN) = seen;
      SB();
      if (n3 > n4 && __builtin_amdgcn_ballot_w64(obs < (n4 >> 2)) == 0) {
        u32x4 of[33]; T4_fetch(of, rw, LAYER == 1, 2 * n4 + (a >> 4), a & 15, w, l);
        asm volatile("s_waitcnt vmcnt(33)" ::: "memory");
        posted();
        project(of);
        continue;
      }
      drain(); posted();
      if (ahead && n3 == n4 + 4) { bar(); continue; }
    }
    await<1>(rw, fob, n4 >> 2, dead);
    u32x4 of[33]; T4_fetch(of, rw, LAYER == 1, 2 * n4 + (a >> 4), a & 15, w, l);
    project(of);
  }
}
DI void b_role(const Params& p, LAS char* lds, int j, int w) {
  const int l = fresh_lane();
  const int h = j >> 2, qd = j & 3;
  for (int T = 0; T < 4; ++T) fill_tile(lds + T * 16384, (qd & 2) ? p.Wv : p.Wk, 4096, 0, 512, h * 128 + (qd & 1) * 64 + 16 * T, w, 4, l);
  for (int T = 0; T < 2; ++T) fill_tile(lds + (4 + T) * 16384, p.Wq, 4096, 0, 512, h * 128 + qd * 32 + 16 * T, w, 4, l);
  drain(); __syncthreads();
  for (int T = w; T < 6; T += 4) *(LAS f32x4*)(lds + B_CS + T * 1024 + l * 16) = ((qd & 2) && T < 4) ? colsum_lane(lds + T * 16384, l) : colsum_reg(lds + T * 16384, l);
  __syncthreads();
  int dead = 0;
#pragma unroll 1
  for (int layer = 0; layer < 2; ++layer) {
    int jj = j, ww = w; asm volatile("" : "+s"(jj), "+s"(ww));
    B_rows(p, lds, layer, jj, ww, l, dead);
  }
}

#define CLOB_H "v12", "v13", "v14", "v15", "v16", "v17", "v18", "v19", "v28", "v29", "v30", "v31", "v32", "v33", "v34", "v35", "v36", "v37", "v38", "v39", "v40", "v41", "v42", "v43", "v44", "v45", "v46", "v47", "v48", "v49", "v50", "v51", "v52", "v53", "v54", "v55", "v56", "v57", "v58", "v59", "v60", "v61", "v62", "v63", "v64", "v65", "v66", "v67", "v68", "v69", "v70", "v71", "v72", "v73", "v74", "v75", "v76", "v77", "v78", "v79", "v80", "v81", "v82", "v83", "v84", "v85", "v86", "v87", "v88", "v89", "v90", "v91", "v92", "v93", "v94", "v95", "v96", "v97", "v98", "v99", "v100", "v101", "v102", "v103", "v104", "v105", "v106", "v107", "v108", "v109", "v110", "v111", "v112", "v113", "v114", "v115", "v116", "v117", "v118", "v119", "v120", "v121", "v122", "v123", "v124", "v125", "v126", "v127", "v128", "v129", "v130", "v131", "v132", "v133", "v134", "v135", "v136", "v137", "v138", "v139", "v140", "v141", "v142", "v143", "v144", "v145", "v146", "v147", "v148", "v149", "v150", "v151", "v152", "v153", "v154", "v155", "v156", "v157", "v158", "v159", "v160", "v161", "v162", "v163", "v164", "v165", "v166", "v167", "v168", "v169", "v170", "v171", "v172", "v173", "v174", "v175", "v176", "v177", "v178", "v179", "v180", "v181", "v182", "v183", "v184", "v185", "v186", "v187", "v188", "v189", "v190", "v191", "v192", "v193", "v194", "v195", "v196", "v197", "v198", "v199", "v200", "v201", "v202", "v203", "v204", "v205", "v206", "v207", "v208", "v209", "v210", "v211", "v212", "v213", "v214", "v215", "v216", "v217", "v218", "v219", "v220", "v221", "v222", "v223", "v224", "v225", "v226", "v227", "v228", "v229", "v230", "v231", "v232", "v233", "v234", "v235", "v236", "v237", "v238", "v239", "v240", "v241", "v242", "v243", "v244", "v245", "v246", "v247", "v248", "v249", "v250", "v251", "v252", "v253", "a0", "a1", "a2", "a3", "a4", "a5", "a6", "a7", "a8", "a9", "a10", "a11", "a12", "a13", "a14", "a15", "a16", "a17", "a18", "a19", "a20", "a21", "a22", "a23", "a24", "a25", "a26", "a27", "a28", "a29", "a30", "a31", "a32", "a33", "a34", "a35", "a36", "a37", "a38", "a39", "a40", "a41", "a42", "a43", "a44", "a45", "a46", "a47", "a48", "a49", "a50", "a51", "a52", "a53", "a54", "a55", "a56", "a57", "a58", "a59", "a60", "a61", "a62", "a63", "a64", "a65", "a66", "a67", "a68", "a69", "a70", "a71", "a72", "a73", "a74", "a75", "a76", "a77", "a78", "a79", "a80", "a81", "a82", "a83", "a84", "a85", "a86", "a87", "a88", "a89", "a90", "a91", "a92", "a93", "a94", "a95", "a96", "a97", "a98", "a99", "a100", "a101", "a102", "a103", "a104", "a105", "a106", "a107", "a108", "a109", "a110", "a111", "a112", "a113", "a114", "a115", "a116", "a117", "a118", "a119", "a120", "a121", "a122", "a123", "a124", "a125", "a126", "a127", "a128", "a129", "a130", "a131", "a132", "a133", "a134", "a135", "a136", "a137", "a138", "a139", "a140", "a141", "a142", "a143", "a144", "a145", "a146", "a147", "a148", "a149", "a150", "a151", "a152", "a153", "a154", "a155", "a156", "a157", "a158", "a159", "a160", "a161", "a162", "a163", "a164", "a165", "a166", "a167", "a168", "a169", "a170", "a171", "a172", "a173", "a174", "a175", "a176", "a177", "a178", "a179", "a180", "a181", "a182", "a183", "a184", "a185", "a186", "a187", "a188", "a189", "a190", "a191", "a192", "a193", "a194", "a195", "a196", "a197", "a198", "a199", "a200", "a201", "a202", "a203", "a204", "a205", "a206", "a207", "a208", "a209", "a210", "a211", "a212", "a213", "a214", "a215", "a216", "a217", "a218", "a219", "a220", "a221", "a222", "a223", "a224", "a225", "a226", "a227", "a228", "a229", "a230", "a231", "a232", "a233", "a234", "a235", "a236", "a237", "a238", "a239", "a240", "a241", "a242", "a243", "a244", "a245", "a246", "a247", "a248", "a249", "a250", "a251", "a252", "a253", "a254", "a255", "s36", "s37", "s38", "s39", "s40", "s41", "s42", "s43", "s44", "s45", "s46", "s47", "s48", "s49", "s50", "s51", "s52", "s53", "s54", "s55", "s56", "s57", "s58", "s59", "s60", "s61", "s62", "s63", "s64", "s65", "s66", "s67", "s68", "s69", "s70", "s71", "s72", "s73", "s74", "s75", "s76", "s77", "s78", "s79"
#define CLOB_R "v8", "v9", "v10", "v11", "v12", "v13", "v14", "v15", "v16", "v17", "v18", "v19", "v20", "v21", "v22", "v23", "v24", "v25", "v26", "v27", "v28", "v29", "v30", "v31", "v32", "v33", "v34", "v35", "v36", "v37", "v38", "v39", "v40", "v41", "v42", "v43", "v44", "v45", "v46", "v47", "v48", "v49", "v50", "v51", "v52", "s59", "s60", "s61", "s62", "s63", "s64", "s65", "s66", "s67", "s68", "s69", "s70", "s71", "s72", "s73", "s74", "s75", "s76", "s77", "s78", "s79"
#define NSPLIT 2
constexpr int NI = 2 / NSPLIT;
constexpr int H_QX = 0, H_KX = 2048, H_OX = 6400, H_WO = 8704, H_WST = NI * 16384 + 4096, H_STAMP = H_WO + 4 * H_WST, H_LDS = 163840, H_STG = H_LDS - 18560;
typedef u32 u32x16 __attribute__((ext_vector_type(16)));
#define DLY_H 0
#define DLY_R 0
DI void head_role(const Params& p, int h, int c, int w) {
  const int l = fresh_lane(), tid = w * 64 + l, g = l >> 4, i16 = l & 15, q4 = i16 >> 2, p4 = l & 3;
  const int row = 2 * (i16 >> 2) + (i16 & 1), ph = (i16 >> 1) & 1;
  u32x8 c0; u32x4 c1; u32x8 c2; u32x16 sc;
  c0[0] = l * 16; c0[1] = ph ? 0xdddd : 0x8888;
  c0[2] = cphys(C_RING + (4 * (w + 4 * c) + g) * 8192 + h * 256 + i16 * 16);
  c0[3] = q4 * 256 + (2 * g + (i16 & 1)) * 32 + ph * 16;
  c0[4] = H_QX + (32 * w + i16) * 16 + 4 * g;
  c0[5] = H_QX + g * 64 + q4 * 16 + (l & 1) * 8;
  c0[6] = H_KX + g * 1088 + (32 * w + i16) * 8;
  c0[7] = H_KX + p4 * 1088 + (4 * g + q4) * 8;
  c1[0] = H_OX + (i16 & 7) * 272 + (32 * w + 2 * (i16 & 8) + 4 * g) * 2;
  c1[1] = H_OX + row * 272 + g * 32 + ph * 16; c1[2] = H_WO + w * H_WST + l * 16; c1[3] = g * 128 + row * 16;
  c2[0] = H_STG + (tid >> 4) * 288 + (tid & 15) * 16;
  c2[1] = H_STG + (16 * (g >> 1) + 4 * (g & 1) + 8 * (q4 & 1) + (q4 >> 1)) * 288 + (32 * w + 4 * p4) * 2;
  c2[2] = 0;
  c2[3] = H_WO + (w >> 1) * H_WST + (w & 1) * 8192 + l * 16; c2[4] = (tid >> 4) * 8192 + (tid & 15) * 16; c2[5] = (tid >> 4) * 1024 + (tid & 15) * 16; c2[6] = 0; c2[7] = 0;
  sc[0] = F_PAR; sc[1] = 128; sc[2] = 0; sc[3] = OOB; sc[4] = F_T1 + h * 2048;
  sc[5] = W_L + L_KX + h * 8192; sc[6] = W_L + L_VX + h * 8192 + w * 2048; sc[7] = cphys(C_MEM);
  sc[8] = W_L + L_KM + h * 2048 + w * 512; sc[9] = W_L + L_VM + h * 4096 + w * 1024; sc[10] = (c == 0 && w == 0) ? F_KVM + h * 128 : OOB; sc[11] = w;
  sc[12] = H_STAMP + w * 4096 - 32; sc[13] = c == 0; sc[14] = w == 0 ? cphys(C_HINT) : OOB; sc[15] = w == 0 ? cphys(C_HINT + (2 * h + c) * 128) : OOB;
#pragma unroll
  for (int i = 0; i < 16; ++i) { const u32 t = sc[i]; sc[i] = __builtin_amdgcn_readfirstlane(t); }
  const rsrc_t rw = mkrsrc(p.ws, W_END), rq = mkrsrc(p.Wq + h * 128, 4 * MB - h * 256), rk = mkrsrc(p.Wk + h * 128, 4 * MB - h * 256), rv = mkrsrc(p.Wv + h * 128, 4 * MB - h * 256), ro = mkrsrc(p.Wo + h * 65536 + c * 256, 131072 - c * 512);
  asm volatile(
"s_mov_b32 s68, 0\n"
"buffer_load_dwordx4 v[192:195], v25, %[rwo], s68 offen\n"
"s_mov_b32 s68, 16384\n"
"buffer_load_dwordx4 v[196:199], v25, %[rwo], s68 offen\n"
"s_mov_b32 s68, 32768\n"
"buffer_load_dwordx4 v[200:203], v25, %[rwo], s68 offen\n"
"s_mov_b32 s68, 49152\n"
"buffer_load_dwordx4 v[204:207], v25, %[rwo], s68 offen\n"
"s_mov_b32 s68, 256\n"
"buffer_load_dwordx4 v[208:211], v25, %[rwo], s68 offen\n"
"s_mov_b32 s68, 16640\n"
"buffer_load_dwordx4 v[212:215], v25, %[rwo], s68 offen\n"
"s_mov_b32 s68, 33024\n"
"buffer_load_dwordx4 v[216:219], v25, %[rwo], s68 offen\n"
"s_mov_b32 s68, 49408\n"
"buffer_load_dwordx4 v[220:223], v25, %[rwo], s68 offen\n"
"s_mov_b32 s68, 65536\n"
"buffer_load_dwordx4 v[224:227], v25, %[rwo], s68 offen\n"
"s_mov_b32 s68, 81920\n"
"buffer_load_dwordx4 v[228:231], v25, %[rwo], s68 offen\n"
"s_mov_b32 s68, 98304\n"
"buffer_load_dwordx4 v[232:235], v25, %[rwo], s68 offen\n"
"s_mov_b32 s68, 114688\n"
"buffer_load_dwordx4 v[236:239], v25, %[rwo], s68 offen\n"
"s_mov_b32 s68, 65792\n"
"buffer_load_dwordx4 v[96:99], v25, %[rwo], s68 offen\n"
"s_mov_b32 s68, 82176\n"
"buffer_load_dwordx4 v[100:103], v25, %[rwo], s68 offen\n"
"s_mov_b32 s68, 98560\n"
"buffer_load_dwordx4 v[104:107], v25, %[rwo], s68 offen\n"
"s_mov_b32 s68, 114944\n"
"buffer_load_dwordx4 v[108:111], v25, %[rwo], s68 offen\n"
"s_mov_b32 s68, 0\n"
"buffer_load_dwordx4 v[128:131], v24, %[rwq], s68 offen\n"
"s_mov_b32 s68, 131072\n"
"buffer_load_dwordx4 v[132:135], v24, %[rwq], s68 offen\n"
"s_mov_b32 s68, 262144\n"
"buffer_load_dwordx4 v[136:139], v24, %[rwq], s68 offen\n"
"s_mov_b32 s68, 393216\n"
"buffer_load_dwordx4 v[140:143], v24, %[rwq], s68 offen\n"
"s_mov_b32 s68, 524288\n"
"buffer_load_dwordx4 v[144:147], v24, %[rwq], s68 offen\n"
"s_mov_b32 s68, 655360\n"
"buffer_load_dwordx4 v[148:151], v24, %[rwq], s68 offen\n"
"s_mov_b32 s68, 786432\n"
"buffer_load_dwordx4 v[152:155], v24, %[rwq], s68 offen\n"
"s_mov_b32 s68, 917504\n"
"buffer_load_dwordx4 v[156:159], v24, %[rwq], s68 offen\n"
"s_mov_b32 s68, 1048576\n"
"buffer_load_dwordx4 v[160:163], v24, %[rwq], s68 offen\n"
"s_mov_b32 s68, 1179648\n"
"buffer_load_dwordx4 v[164:167], v24, %[rwq], s68 offen\n"
"s_mov_b32 s68, 1310720\n"
"buffer_load_dwordx4 v[168:171], v24, %[rwq], s68 offen\n"
"s_mov_b32 s68, 1441792\n"
"buffer_load_dwordx4 v[172:175], v24, %[rwq], s68 offen\n"
"s_mov_b32 s68, 1572864\n"
"buffer_load_dwordx4 v[176:179], v24, %[rwq], s68 offen\n"
"s_mov_b32 s68, 1703936\n"
"buffer_load_dwordx4 v[180:183], v24, %[rwq], s68 offen\n"
"s_mov_b32 s68, 1835008\n"
"buffer_load_dwordx4 v[184:187], v24, %[rwq], s68 offen\n"
"s_mov_b32 s68, 1966080\n"
"buffer_load_dwordx4 v[188:191], v24, %[rwq], s68 offen\n"
"s_waitcnt vmcnt(31)\n"
"ds_write_b128 v20, v[192:195]\n"
"s_waitcnt vmcnt(30)\n"
"ds_write_b128 v20, v[196:199] offset:4608\n"
"s_waitcnt vmcnt(29)\n"
"ds_write_b128 v20, v[200:203] offset:9344\n"
"s_waitcnt vmcnt(28)\n"
"ds_write_b128 v20, v[204:207] offset:13952\n"
"s_waitcnt lgkmcnt(0)\n"
"s_barrier\n"
"s_mov_b32 s68, 2097152\n"
"buffer_load_dwordx4 v[192:195], v24, %[rwq], s68 offen\n"
"s_mov_b32 s68, 2228224\n"
"buffer_load_dwordx4 v[196:199], v24, %[rwq], s68 offen\n"
"s_mov_b32 s68, 2359296\n"
"buffer_load_dwordx4 v[200:203], v24, %[rwq], s68 offen\n"
"s_mov_b32 s68, 2490368\n"
"buffer_load_dwordx4 v[204:207], v24, %[rwq], s68 offen\n"
"ds_read_b64_tr_b16 v[32:33], v21\n"
"ds_read_b64_tr_b16 v[34:35], v21 offset:576\n"
"ds_read_b64_tr_b16 v[36:37], v21 offset:9344\n"
"ds_read_b64_tr_b16 v[38:39], v21 offset:9920\n"
"ds_read_b64_tr_b16 v[40:41], v21 offset:32\n"
"ds_read_b64_tr_b16 v[42:43], v21 offset:608\n"
"ds_read_b64_tr_b16 v[44:45], v21 offset:9376\n"
"ds_read_b64_tr_b16 v[46:47], v21 offset:9952\n"
"v_add_u32 v12, 0, v23\n"
"s_waitcnt lgkmcnt(6)\n"
"ds_write_b128 v12, v[32:35]\n"
"s_waitcnt lgkmcnt(5)\n"
"ds_write_b128 v12, v[36:39] offset:1024\n"
"s_waitcnt lgkmcnt(4)\n"
"ds_write_b128 v12, v[40:43] offset:4096\n"
"s_waitcnt lgkmcnt(3)\n"
"ds_write_b128 v12, v[44:47] offset:5120\n"
"s_waitcnt lgkmcnt(0)\n"
"s_barrier\n"
"s_waitcnt vmcnt(31)\n"
"ds_write_b128 v20, v[208:211]\n"
"s_waitcnt vmcnt(30)\n"
"ds_write_b128 v20, v[212:215] offset:4608\n"
"s_waitcnt vmcnt(29)\n"
"ds_write_b128 v20, v[216:219] offset:9344\n"
"s_waitcnt vmcnt(28)\n"
"ds_write_b128 v20, v[220:223] offset:13952\n"
"s_waitcnt lgkmcnt(0)\n"
"s_barrier\n"
"s_mov_b32 s68, 2621440\n"
"buffer_load_dwordx4 v[208:211], v24, %[rwq], s68 offen\n"
"s_mov_b32 s68, 2752512\n"
"buffer_load_dwordx4 v[212:215], v24, %[rwq], s68 offen\n"
"s_mov_b32 s68, 2883584\n"
"buffer_load_dwordx4 v[216:219], v24, %[rwq], s68 offen\n"
"s_mov_b32 s68, 3014656\n"
"buffer_load_dwordx4 v[220:223], v24, %[rwq], s68 offen\n"
"ds_read_b64_tr_b16 v[32:33], v21\n"
"ds_read_b64_tr_b16 v[34:35], v21 offset:576\n"
"ds_read_b64_tr_b16 v[36:37], v21 offset:9344\n"
"ds_read_b64_tr_b16 v[38:39], v21 offset:9920\n"
"ds_read_b64_tr_b16 v[40:41], v21 offset:32\n"
"ds_read_b64_tr_b16 v[42:43], v21 offset:608\n"
"ds_read_b64_tr_b16 v[44:45], v21 offset:9376\n"
"ds_read_b64_tr_b16 v[46:47], v21 offset:9952\n"
"v_add_u32 v12, 40960, v23\n"
"s_waitcnt lgkmcnt(6)\n"
"ds_write_b128 v12, v[32:35]\n"
"s_waitcnt lgkmcnt(5)\n"
"ds_write_b128 v12, v[36:39] offset:1024\n"
"s_waitcnt lgkmcnt(4)\n"
"ds_write_b128 v12, v[40:43] offset:4096\n"
"s_waitcnt lgkmcnt(3)\n"
"ds_write_b128 v12, v[44:47] offset:5120\n"
"s_waitcnt lgkmcnt(0)\n"
"s_barrier\n"
"s_waitcnt vmcnt(31)\n"
"ds_write_b128 v20, v[224:227]\n"
"s_waitcnt vmcnt(30)\n"
"ds_write_b128 v20, v[228:231] offset:4608\n"
"s_waitcnt vmcnt(29)\n"
"ds_write_b128 v20, v[232:235] offset:9344\n"
"s_waitcnt vmcnt(28)\n"
"ds_write_b128 v20, v[236:239] offset:13952\n"
"s_waitcnt lgkmcnt(0)\n"
"s_barrier\n"
"s_mov_b32 s68, 3145728\n"
"buffer_load_dwordx4 v[224:227], v24, %[rwq], s68 offen\n"
"s_mov_b32 s68, 3276800\n"
"buffer_load_dwordx4 v[228:231], v24, %[rwq], s68 offen\n"
"s_mov_b32 s68, 3407872\n"
"buffer_load_dwordx4 v[232:235], v24, %[rwq], s68 offen\n"
"s_mov_b32 s68, 3538944\n"
"buffer_load_dwordx4 v[236:239], v24, %[rwq], s68 offen\n"
"ds_read_b64_tr_b16 v[32:33], v21\n"
"ds_read_b64_tr_b16 v[34:35], v21 offset:576\n"
"ds_read_b64_tr_b16 v[36:37], v21 offset:9344\n"
"ds_read_b64_tr_b16 v[38:39], v21 offset:9920\n"
"ds_read_b64_tr_b16 v[40:41], v21 offset:32\n"
"ds_read_b64_tr_b16 v[42:43], v21 offset:608\n"
"ds_read_b64_tr_b16 v[44:45], v21 offset:9376\n"
"ds_read_b64_tr_b16 v[46:47], v21 offset:9952\n"
"v_add_u32 v12, 2048, v23\n"
"s_waitcnt lgkmcnt(6)\n"
"ds_write_b128 v12, v[32:35]\n"
"s_waitcnt lgkmcnt(5)\n"
"ds_write_b128 v12, v[36:39] offset:1024\n"
"s_waitcnt lgkmcnt(4)\n"
"ds_write_b128 v12, v[40:43] offset:4096\n"
"s_waitcnt lgkmcnt(3)\n"
"ds_write_b128 v12, v[44:47] offset:5120\n"
"s_waitcnt lgkmcnt(0)\n"
"s_barrier\n"
"s_waitcnt vmcnt(31)\n"
"ds_write_b128 v20, v[96:99]\n"
"s_waitcnt vmcnt(30)\n"
"ds_write_b128 v20, v[100:103] offset:4608\n"
"s_waitcnt vmcnt(29)\n"
"ds_write_b128 v20, v[104:107] offset:9344\n"
"s_waitcnt vmcnt(28)\n"
"ds_write_b128 v20, v[108:111] offset:13952\n"
"s_waitcnt lgkmcnt(0)\n"
"s_barrier\n"
"s_mov_b32 s68, 3670016\n"
"buffer_load_dwordx4 v[96:99], v24, %[rwq], s68 offen\n"
"s_mov_b32 s68, 3801088\n"
"buffer_load_dwordx4 v[100:103], v24, %[rwq], s68 offen\n"
"s_mov_b32 s68, 3932160\n"
"buffer_load_dwordx4 v[104:107], v24, %[rwq], s68 offen\n"
"s_mov_b32 s68, 4063232\n"
"buffer_load_dwordx4 v[108:111], v24, %[rwq], s68 offen\n"
"ds_read_b64_tr_b16 v[32:33], v21\n"
"ds_read_b64_tr_b16 v[34:35], v21 offset:576\n"
"ds_read_b64_tr_b16 v[36:37], v21 offset:9344\n"
"ds_read_b64_tr_b16 v[38:39], v21 offset:9920\n"
"ds_read_b64_tr_b16 v[40:41], v21 offset:32\n"
"ds_read_b64_tr_b16 v[42:43], v21 offset:608\n"
"ds_read_b64_tr_b16 v[44:45], v21 offset:9376\n"
"ds_read_b64_tr_b16 v[46:47], v21 offset:9952\n"
"v_add_u32 v12, 43008, v23\n"
"s_waitcnt lgkmcnt(6)\n"
"ds_write_b128 v12, v[32:35]\n"
"s_waitcnt lgkmcnt(5)\n"
"ds_write_b128 v12, v[36:39] offset:1024\n"
"s_waitcnt lgkmcnt(4)\n"
"ds_write_b128 v12, v[40:43] offset:4096\n"
"s_waitcnt lgkmcnt(3)\n"
"ds_write_b128 v12, v[44:47] offset:5120\n"
"s_waitcnt lgkmcnt(0)\n"
"s_barrier\n"
"s_waitcnt vmcnt(31)\n"
"ds_write_b128 v20, v[128:131]\n"
"s_waitcnt vmcnt(30)\n"
"ds_write_b128 v20, v[132:135] offset:4608\n"
"s_waitcnt vmcnt(29)\n"
"ds_write_b128 v20, v[136:139] offset:9344\n"
"s_waitcnt vmcnt(28)\n"
"ds_write_b128 v20, v[140:143] offset:13952\n"
"s_waitcnt lgkmcnt(0)\n"
"s_barrier\n"
"s_mov_b32 s68, 0\n"
"buffer_load_dwordx4 v[128:131], v24, %[rwk], s68 offen\n"
"s_mov_b32 s68, 131072\n"
"buffer_load_dwordx4 v[132:135], v24, %[rwk], s68 offen\n"
"s_mov_b32 s68, 262144\n"
"buffer_load_dwordx4 v[136:139], v24, %[rwk], s68 offen\n"
"s_mov_b32 s68, 393216\n"
"buffer_load_dwordx4 v[140:143], v24, %[rwk], s68 offen\n"
"ds_read_b64_tr_b16 a[0:1], v21\n"
"ds_read_b64_tr_b16 a[2:3], v21 offset:576\n"
"ds_read_b64_tr_b16 a[4:5], v21 offset:9344\n"
"ds_read_b64_tr_b16 a[6:7], v21 offset:9920\n"
"ds_read_b64_tr_b16 a[8:9], v21 offset:32\n"
"ds_read_b64_tr_b16 a[10:11], v21 offset:608\n"
"ds_read_b64_tr_b16 a[12:13], v21 offset:9376\n"
"ds_read_b64_tr_b16 a[14:15], v21 offset:9952\n"
"s_waitcnt lgkmcnt(0)\n"
"s_barrier\n"
"s_waitcnt vmcnt(31)\n"
"ds_write_b128 v20, v[144:147]\n"
"s_waitcnt vmcnt(30)\n"
"ds_write_b128 v20, v[148:151] offset:4608\n"
"s_waitcnt vmcnt(29)\n"
"ds_write_b128 v20, v[152:155] offset:9344\n"
"s_waitcnt vmcnt(28)\n"
"ds_write_b128 v20, v[156:159] offset:13952\n"
"s_waitcnt lgkmcnt(0)\n"
"s_barrier\n"
"s_mov_b32 s68, 524288\n"
"buffer_load_dwordx4 v[144:147], v24, %[rwk], s68 offen\n"
"s_mov_b32 s68, 655360\n"
"buffer_load_dwordx4 v[148:151], v24, %[rwk], s68 offen\n"
"s_mov_b32 s68, 786432\n"
"buffer_load_dwordx4 v[152:155], v24, %[rwk], s68 offen\n"
"s_mov_b32 s68, 917504\n"
"buffer_load_dwordx4 v[156:159], v24, %[rwk], s68 offen\n"
"ds_read_b64_tr_b16 a[16:17], v21\n"
"ds_read_b64_tr_b16 a[18:19], v21 offset:576\n"
"ds_read_b64_tr_b16 a[20:21], v21 offset:9344\n"
"ds_read_b64_tr_b16 a[22:23], v21 offset:9920\n"
"ds_read_b64_tr_b16 a[24:25], v21 offset:32\n"
"ds_read_b64_tr_b16 a[26:27], v21 offset:608\n"
"ds_read_b64_tr_b16 a[28:29], v21 offset:9376\n"
"ds_read_b64_tr_b16 a[30:31], v21 offset:9952\n"
"s_waitcnt lgkmcnt(0)\n"
"s_barrier\n"
"s_waitcnt vmcnt(31)\n"
"ds_write_b128 v20, v[160:163]\n"
"s_waitcnt vmcnt(30)\n"
"ds_write_b128 v20, v[164:167] offset:4608\n"
"s_waitcnt vmcnt(29)\n"
"ds_write_b128 v20, v[168:171] offset:9344\n"
"s_waitcnt vmcnt(28)\n"
"ds_write_b128 v20, v[172:175] offset:13952\n"
"s_waitcnt lgkmcnt(0)\n"
"s_barrier\n"
"s_mov_b32 s68, 1048576\n"
"buffer_load_dwordx4 v[160:163], v24, %[rwk], s68 offen\n"
"s_mov_b32 s68, 1179648\n"
"buffer_load_dwordx4 v[164:167], v24, %[rwk], s68 offen\n"
"s_mov_b32 s68, 1310720\n"
"buffer_load_dwordx4 v[168:171], v24, %[rwk], s68 offen\n"
"s_mov_b32 s68, 1441792\n"
"buffer_load_dwordx4 v[172:175], v24, %[rwk], s68 offen\n"
"ds_read_b64_tr_b16 a[32:33], v21\n"
"ds_read_b64_tr_b16 a[34:35], v21 offset:576\n"
"ds_read_b64_tr_b16 a[36:37], v21 offset:9344\n"
"ds_read_b64_tr_b16 a[38:39], v21 offset:9920\n"
"ds_read_b64_tr_b16 a[40:41], v21 offset:32\n"
"ds_read_b64_tr_b16 a[42:43], v21 offset:608\n"
"ds_read_b64_tr_b16 a[44:45], v21 offset:9376\n"
"ds_read_b64_tr_b16 a[46:47], v21 offset:9952\n"
"s_waitcnt lgkmcnt(0)\n"
"s_barrier\n"
"s_waitcnt vmcnt(31)\n"
"ds_write_b128 v20, v[176:179]\n"
"s_waitcnt vmcnt(30)\n"
"ds_write_b128 v20, v[180:183] offset:4608\n"
"s_waitcnt vmcnt(29)\n"
"ds_write_b128 v20, v[184:187] offset:9344\n"
"s_waitcnt vmcnt(28)\n"
"ds_write_b128 v20, v[188:191] offset:13952\n"
"s_waitcnt lgkmcnt(0)\n"
"s_barrier\n"
"s_mov_b32 s68, 1572864\n"
"buffer_load_dwordx4 v[176:179], v24, %[rwk], s68 offen\n"
"s_mov_b32 s68, 1703936\n"
"buffer_load_dwordx4 v[180:183], v24, %[rwk], s68 offen\n"
"s_mov_b32 s68, 1835008\n"
"buffer_load_dwordx4 v[184:187], v24, %[rwk], s68 offen\n"
"s_mov_b32 s68, 1966080\n"
"buffer_load_dwordx4 v[188:191], v24, %[rwk], s68 offen\n"
"ds_read_b64_tr_b16 a[48:49], v21\n"
"ds_read_b64_tr_b16 a[50:51], v21 offset:576\n"
"ds_read_b64_tr_b16 a[52:53], v21 offset:9344\n"
"ds_read_b64_tr_b16 a[54:55], v21 offset:9920\n"
"ds_read_b64_tr_b16 a[56:57], v21 offset:32\n"
"ds_read_b64_tr_b16 a[58:59], v21 offset:608\n"
"ds_read_b64_tr_b16 a[60:61], v21 offset:9376\n"
"ds_read_b64_tr_b16 a[62:63], v21 offset:9952\n"
"s_waitcnt lgkmcnt(0)\n"
"s_barrier\n"
"s_waitcnt vmcnt(31)\n"
"ds_write_b128 v20, v[192:195]\n"
"s_waitcnt vmcnt(30)\n"
"ds_write_b128 v20, v[196:199] offset:4608\n"
"s_waitcnt vmcnt(29)\n"
"ds_write_b128 v20, v[200:203] offset:9344\n"
"s_waitcnt vmcnt(28)\n"
"ds_write_b128 v20, v[204:207] offset:13952\n"
"s_waitcnt lgkmcnt(0)\n"
"s_barrier\n"
"s_mov_b32 s68, 2097152\n"
"buffer_load_dwordx4 v[192:195], v24, %[rwk], s68 offen\n"
"s_mov_b32 s68, 2228224\n"
"buffer_load_dwordx4 v[196:199], v24, %[rwk], s68 offen\n"
"s_mov_b32 s68, 2359296\n"
"buffer_load_dwordx4 v[200:203], v24, %[rwk], s68 offen\n"
"s_mov_b32 s68, 2490368\n"
"buffer_load_dwordx4 v[204:207], v24, %[rwk], s68 offen\n"
"ds_read_b64_tr_b16 a[64:65], v21\n"
"ds_read_b64_tr_b16 a[66:67], v21 offset:576\n"
"ds_read_b64_tr_b16 a[68:69], v21 offset:9344\n"
"ds_read_b64_tr_b16 a[70:71], v21 offset:9920\n"
"ds_read_b64_tr_b16 a[72:73], v21 offset:32\n"
"ds_read_b64_tr_b16 a[74:75], v21 offset:608\n"
"ds_read_b64_tr_b16 a[76:77], v21 offset:9376\n"
"ds_read_b64_tr_b16 a[78:79], v21 offset:9952\n"
"s_waitcnt lgkmcnt(0)\n"
"s_barrier\n"
"s_waitcnt vmcnt(31)\n"
"ds_write_b128 v20, v[208:211]\n"
"s_waitcnt vmcnt(30)\n"
"ds_write_b128 v20, v[212:215] offset:4608\n"
"s_waitcnt vmcnt(29)\n"
"ds_write_b128 v20, v[216:219] offset:9344\n"
"s_waitcnt vmcnt(28)\n"
"ds_write_b128 v20, v[220:223] offset:13952\n"
"s_waitcnt lgkmcnt(0)\n"
"s_barrier\n"
"s_mov_b32 s68, 2621440\n"
"buffer_load_dwordx4 v[208:211], v24, %[rwk], s68 offen\n"
"s_mov_b32 s68, 2752512\n"
"buffer_load_dwordx4 v[212:215], v24, %[rwk], s68 offen\n"
"s_mov_b32 s68, 2883584\n"
"buffer_load_dwordx4 v[216:219], v24, %[rwk], s68 offen\n"
"s_mov_b32 s68, 3014656\n"
"buffer_load_dwordx4 v[220:223], v24, %[rwk], s68 offen\n"
"ds_read_b64_tr_b16 a[80:81], v21\n"
"ds_read_b64_tr_b16 a[82:83], v21 offset:576\n"
"ds_read_b64_tr_b16 a[84:85], v21 offset:9344\n"
"ds_read_b64_tr_b16 a[86:87], v21 offset:9920\n"
"ds_read_b64_tr_b16 a[88:89], v21 offset:32\n"
"ds_read_b64_tr_b16 a[90:91], v21 offset:608\n"
"ds_read_b64_tr_b16 a[92:93], v21 offset:9376\n"
"ds_read_b64_tr_b16 a[94:95], v21 offset:9952\n"
"s_waitcnt lgkmcnt(0)\n"
"s_barrier\n"
"s_waitcnt vmcnt(31)\n"
"ds_write_b128 v20, v[224:227]\n"
"s_waitcnt vmcnt(30)\n"
"ds_write_b128 v20, v[228:231] offset:4608\n"
"s_waitcnt vmcnt(29)\n"
"ds_write_b128 v20, v[232:235] offset:9344\n"
"s_waitcnt vmcnt(28)\n"
"ds_write_b128 v20, v[236:239] offset:13952\n"
"s_waitcnt lgkmcnt(0)\n"
"s_barrier\n"
"s_mov_b32 s68, 3145728\n"
"buffer_load_dwordx4 v[224:227], v24, %[rwk], s68 offen\n"
"s_mov_b32 s68, 3276800\n"
"buffer_load_dwordx4 v[228:231], v24, %[rwk], s68 offen\n"
"s_mov_b32 s68, 3407872\n"
"buffer_load_dwordx4 v[232:235], v24, %[rwk], s68 offen\n"
"s_mov_b32 s68, 3538944\n"
"buffer_load_dwordx4 v[236:239], v24, %[rwk], s68 offen\n"
"ds_read_b64_tr_b16 a[96:97], v21\n"
"ds_read_b64_tr_b16 a[98:99], v21 offset:576\n"
"ds_read_b64_tr_b16 a[100:101], v21 offset:9344\n"
"ds_read_b64_tr_b16 a[102:103], v21 offset:9920\n"
"ds_read_b64_tr_b16 a[104:105], v21 offset:32\n"
"ds_read_b64_tr_b16 a[106:107], v21 offset:608\n"
"ds_read_b64_tr_b16 a[108:109], v21 offset:9376\n"
"ds_read_b64_tr_b16 a[110:111], v21 offset:9952\n"
"s_waitcnt lgkmcnt(0)\n"
"s_barrier\n"
"s_waitcnt vmcnt(31)\n"
"ds_write_b128 v20, v[96:99]\n"
"s_waitcnt vmcnt(30)\n"
"ds_write_b128 v20, v[100:103] offset:4608\n"
"s_waitcnt vmcnt(29)\n"
"ds_write_b128 v20, v[104:107] offset:9344\n"
"s_waitcnt vmcnt(28)\n"
"ds_write_b128 v20, v[108:111] offset:13952\n"
"s_waitcnt lgkmcnt(0)\n"
"s_barrier\n"
"s_mov_b32 s68, 3670016\n"
"buffer_load_dwordx4 v[96:99], v24, %[rwk], s68 offen\n"
"s_mov_b32 s68, 3801088\n"
"buffer_load_dwordx4 v[100:103], v24, %[rwk], s68 offen\n"
"s_mov_b32 s68, 3932160\n"
"buffer_load_dwordx4 v[104:107], v24, %[rwk], s68 offen\n"
"s_mov_b32 s68, 4063232\n"
"buffer_load_dwordx4 v[108:111], v24, %[rwk], s68 offen\n"
"ds_read_b64_tr_b16 a[112:113], v21\n"
"ds_read_b64_tr_b16 a[114:115], v21 offset:576\n"
"ds_read_b64_tr_b16 a[116:117], v21 offset:9344\n"
"ds_read_b64_tr_b16 a[118:119], v21 offset:9920\n"
"ds_read_b64_tr_b16 a[120:121], v21 offset:32\n"
"ds_read_b64_tr_b16 a[122:123], v21 offset:608\n"
"ds_read_b64_tr_b16 a[124:125], v21 offset:9376\n"
"ds_read_b64_tr_b16 a[126:127], v21 offset:9952\n"
"s_waitcnt lgkmcnt(0)\n"
"s_barrier\n"
"s_waitcnt vmcnt(31)\n"
"ds_write_b128 v20, v[128:131]\n"
"s_waitcnt vmcnt(30)\n"
"ds_write_b128 v20, v[132:135] offset:4608\n"
"s_waitcnt vmcnt(29)\n"
"ds_write_b128 v20, v[136:139] offset:9344\n"
"s_waitcnt vmcnt(28)\n"
"ds_write_b128 v20, v[140:143] offset:13952\n"
"s_waitcnt lgkmcnt(0)\n"
"s_barrier\n"
"s_mov_b32 s68, 0\n"
"buffer_load_dwordx4 v[128:131], v24, %[rwv], s68 offen\n"
"s_mov_b32 s68, 131072\n"
"buffer_load_dwordx4 v[132:135], v24, %[rwv], s68 offen\n"
"s_mov_b32 s68, 262144\n"
"buffer_load_dwordx4 v[136:139], v24, %[rwv], s68 offen\n"
"s_mov_b32 s68, 393216\n"
"buffer_load_dwordx4 v[140:143], v24, %[rwv], s68 offen\n"
"ds_read_b64_tr_b16 a[128:129], v21\n"
"ds_read_b64_tr_b16 a[130:131], v21 offset:576\n"
"ds_read_b64_tr_b16 a[132:133], v21 offset:9344\n"
"ds_read_b64_tr_b16 a[134:135], v21 offset:9920\n"
"ds_read_b64_tr_b16 a[136:137], v21 offset:32\n"
"ds_read_b64_tr_b16 a[138:139], v21 offset:608\n"
"ds_read_b64_tr_b16 a[140:141], v21 offset:9376\n"
"ds_read_b64_tr_b16 a[142:143], v21 offset:9952\n"
"s_waitcnt lgkmcnt(0)\n"
"s_barrier\n"
"s_waitcnt vmcnt(31)\n"
"ds_write_b128 v20, v[144:147]\n"
"s_waitcnt vmcnt(30)\n"
"ds_write_b128 v20, v[148:151] offset:4608\n"
"s_waitcnt vmcnt(29)\n"
"ds_write_b128 v20, v[152:155] offset:9344\n"
"s_waitcnt vmcnt(28)\n"
"ds_write_b128 v20, v[156:159] offset:13952\n"
"s_waitcnt lgkmcnt(0)\n"
"s_barrier\n"
"s_mov_b32 s68, 524288\n"
"buffer_load_dwordx4 v[144:147], v24, %[rwv], s68 offen\n"
"s_mov_b32 s68, 655360\n"
"buffer_load_dwordx4 v[148:151], v24, %[rwv], s68 offen\n"
"s_mov_b32 s68, 786432\n"
"buffer_load_dwordx4 v[152:155], v24, %[rwv], s68 offen\n"
"s_mov_b32 s68, 917504\n"
"buffer_load_dwordx4 v[156:159], v24, %[rwv], s68 offen\n"
"ds_read_b64_tr_b16 a[144:145], v21\n"
"ds_read_b64_tr_b16 a[146:147], v21 offset:576\n"
"ds_read_b64_tr_b16 a[148:149], v21 offset:9344\n"
"ds_read_b64_tr_b16 a[150:151], v21 offset:9920\n"
"ds_read_b64_tr_b16 a[152:153], v21 offset:32\n"
"ds_read_b64_tr_b16 a[154:155], v21 offset:608\n"
"ds_read_b64_tr_b16 a[156:157], v21 offset:9376\n"
"ds_read_b64_tr_b16 a[158:159], v21 offset:9952\n"
"s_waitcnt lgkmcnt(0)\n"
"s_barrier\n"
"s_waitcnt vmcnt(31)\n"
"ds_write_b128 v20, v[160:163]\n"
"s_waitcnt vmcnt(30)\n"
"ds_write_b128 v20, v[164:167] offset:4608\n"
"s_waitcnt vmcnt(29)\n"
"ds_write_b128 v20, v[168:171] offset:9344\n"
"s_waitcnt vmcnt(28)\n"
"ds_write_b128 v20, v[172:175] offset:13952\n"
"s_waitcnt lgkmcnt(0)\n"
"s_barrier\n"
"s_mov_b32 s68, 1048576\n"
"buffer_load_dwordx4 v[160:163], v24, %[rwv], s68 offen\n"
"s_mov_b32 s68, 1179648\n"
"buffer_load_dwordx4 v[164:167], v24, %[rwv], s68 offen\n"
"s_mov_b32 s68, 1310720\n"
"buffer_load_dwordx4 v[168:171], v24, %[rwv], s68 offen\n"
"s_mov_b32 s68, 1441792\n"
"buffer_load_dwordx4 v[172:175], v24, %[rwv], s68 offen\n"
"ds_read_b64_tr_b16 a[160:161], v21\n"
"ds_read_b64_tr_b16 a[162:163], v21 offset:576\n"
"ds_read_b64_tr_b16 a[164:165], v21 offset:9344\n"
"ds_read_b64_tr_b16 a[166:167], v21 offset:9920\n"
"ds_read_b64_tr_b16 a[168:169], v21 offset:32\n"
"ds_read_b64_tr_b16 a[170:171], v21 offset:608\n"
"ds_read_b64_tr_b16 a[172:173], v21 offset:9376\n"
"ds_read_b64_tr_b16 a[174:175], v21 offset:9952\n"
"s_waitcnt lgkmcnt(0)\n"
"s_barrier\n"
"s_waitcnt vmcnt(31)\n"
"ds_write_b128 v20, v[176:179]\n"
"s_waitcnt vmcnt(30)\n"
"ds_write_b128 v20, v[180:183] offset:4608\n"
"s_waitcnt vmcnt(29)\n"
"ds_write_b128 v20, v[184:187] offset:9344\n"
"s_waitcnt vmcnt(28)\n"
"ds_write_b128 v20, v[188:191] offset:13952\n"
"s_waitcnt lgkmcnt(0)\n"
"s_barrier\n"
"s_mov_b32 s68, 1572864\n"
"buffer_load_dwordx4 v[176:179], v24, %[rwv], s68 offen\n"
"s_mov_b32 s68, 1703936\n"
"buffer_load_dwordx4 v[180:183], v24, %[rwv], s68 offen\n"
"s_mov_b32 s68, 1835008\n"
"buffer_load_dwordx4 v[184:187], v24, %[rwv], s68 offen\n"
"s_mov_b32 s68, 1966080\n"
"buffer_load_dwordx4 v[188:191], v24, %[rwv], s68 offen\n"
"ds_read_b64_tr_b16 a[176:177], v21\n"
"ds_read_b64_tr_b16 a[178:179], v21 offset:576\n"
"ds_read_b64_tr_b16 a[180:181], v21 offset:9344\n"
"ds_read_b64_tr_b16 a[182:183], v21 offset:9920\n"
"ds_read_b64_tr_b16 a[184:185], v21 offset:32\n"
"ds_read_b64_tr_b16 a[186:187], v21 offset:608\n"
"ds_read_b64_tr_b16 a[188:189], v21 offset:9376\n"
"ds_read_b64_tr_b16 a[190:191], v21 offset:9952\n"
"s_waitcnt lgkmcnt(0)\n"
"s_barrier\n"
"s_waitcnt vmcnt(31)\n"
"ds_write_b128 v20, v[192:195]\n"
"s_waitcnt vmcnt(30)\n"
"ds_write_b128 v20, v[196:199] offset:4608\n"
"s_waitcnt vmcnt(29)\n"
"ds_write_b128 v20, v[200:203] offset:9344\n"
"s_waitcnt vmcnt(28)\n"
"ds_write_b128 v20, v[204:207] offset:13952\n"
"s_waitcnt lgkmcnt(0)\n"
"s_barrier\n"
"s_mov_b32 s68, 2097152\n"
"buffer_load_dwordx4 v[192:195], v24, %[rwv], s68 offen\n"
"s_mov_b32 s68, 2228224\n"
"buffer_load_dwordx4 v[196:199], v24, %[rwv], s68 offen\n"
"s_mov_b32 s68, 2359296\n"
"buffer_load_dwordx4 v[200:203], v24, %[rwv], s68 offen\n"
"s_mov_b32 s68, 2490368\n"
"buffer_load_dwordx4 v[204:207], v24, %[rwv], s68 offen\n"
"ds_read_b64_tr_b16 a[192:193], v21\n"
"ds_read_b64_tr_b16 a[194:195], v21 offset:576\n"
"ds_read_b64_tr_b16 a[196:197], v21 offset:9344\n"
"ds_read_b64_tr_b16 a[198:199], v21 offset:9920\n"
"ds_read_b64_tr_b16 a[200:201], v21 offset:32\n"
"ds_read_b64_tr_b16 a[202:203], v21 offset:608\n"
"ds_read_b64_tr_b16 a[204:205], v21 offset:9376\n"
"ds_read_b64_tr_b16 a[206:207], v21 offset:9952\n"
"s_waitcnt lgkmcnt(0)\n"
"s_barrier\n"
"s_waitcnt vmcnt(31)\n"
"ds_write_b128 v20, v[208:211]\n"
"s_waitcnt vmcnt(30)\n"
"ds_write_b128 v20, v[212:215] offset:4608\n"
"s_waitcnt vmcnt(29)\n"
"ds_write_b128 v20, v[216:219] offset:9344\n"
"s_waitcnt vmcnt(28)\n"
"ds_write_b128 v20, v[220:223] offset:13952\n"
"s_waitcnt lgkmcnt(0)\n"
"s_barrier\n"
"s_mov_b32 s68, 2621440\n"
"buffer_load_dwordx4 v[208:211], v24, %[rwv], s68 offen\n"
"s_mov_b32 s68, 2752512\n"
"buffer_load_dwordx4 v[212:215], v24, %[rwv], s68 offen\n"
"s_mov_b32 s68, 2883584\n"
"buffer_load_dwordx4 v[216:219], v24, %[rwv], s68 offen\n"
"s_mov_b32 s68, 3014656\n"
"buffer_load_dwordx4 v[220:223], v24, %[rwv], s68 offen\n"
"ds_read_b64_tr_b16 a[208:209], v21\n"
"ds_read_b64_tr_b16 a[210:211], v21 offset:576\n"
"ds_read_b64_tr_b16 a[212:213], v21 offset:9344\n"
"ds_read_b64_tr_b16 a[214:215], v21 offset:9920\n"
"ds_read_b64_tr_b16 a[216:217], v21 offset:32\n"
"ds_read_b64_tr_b16 a[218:219], v21 offset:608\n"
"ds_read_b64_tr_b16 a[220:221], v21 offset:9376\n"
"ds_read_b64_tr_b16 a[222:223], v21 offset:9952\n"
"s_waitcnt lgkmcnt(0)\n"
"s_barrier\n"
"s_waitcnt vmcnt(31)\n"
"ds_write_b128 v20, v[224:227]\n"
"s_waitcnt vmcnt(30)\n"
"ds_write_b128 v20, v[228:231] offset:4608\n"
"s_waitcnt vmcnt(29)\n"
"ds_write_b128 v20, v[232:235] offset:9344\n"
"s_waitcnt vmcnt(28)\n"
"ds_write_b128 v20, v[236:239] offset:13952\n"
"s_waitcnt lgkmcnt(0)\n"
"s_barrier\n"
"s_mov_b32 s68, 3145728\n"
"buffer_load_dwordx4 v[224:227], v24, %[rwv], s68 offen\n"
"s_mov_b32 s68, 3276800\n"
"buffer_load_dwordx4 v[228:231], v24, %[rwv], s68 offen\n"
"s_mov_b32 s68, 3407872\n"
"buffer_load_dwordx4 v[232:235], v24, %[rwv], s68 offen\n"
"s_mov_b32 s68, 3538944\n"
"buffer_load_dwordx4 v[236:239], v24, %[rwv], s68 offen\n"
"ds_read_b64_tr_b16 a[224:225], v21\n"
"ds_read_b64_tr_b16 a[226:227], v21 offset:576\n"
"ds_read_b64_tr_b16 a[228:229], v21 offset:9344\n"
"ds_read_b64_tr_b16 a[230:231], v21 offset:9920\n"
"ds_read_b64_tr_b16 a[232:233], v21 offset:32\n"
"ds_read_b64_tr_b16 a[234:235], v21 offset:608\n"
"ds_read_b64_tr_b16 a[236:237], v21 offset:9376\n"
"ds_read_b64_tr_b16 a[238:239], v21 offset:9952\n"
"s_waitcnt lgkmcnt(0)\n"
"s_barrier\n"
"s_waitcnt vmcnt(31)\n"
"ds_write_b128 v20, v[96:99]\n"
"s_waitcnt vmcnt(30)\n"
"ds_write_b128 v20, v[100:103] offset:4608\n"
"s_waitcnt vmcnt(29)\n"
"ds_write_b128 v20, v[104:107] offset:9344\n"
"s_waitcnt vmcnt(28)\n"
"ds_write_b128 v20, v[108:111] offset:13952\n"
"s_waitcnt lgkmcnt(0)\n"
"s_barrier\n"
"s_mov_b32 s68, 3670016\n"
"buffer_load_dwordx4 v[96:99], v24, %[rwv], s68 offen\n"
"s_mov_b32 s68, 3801088\n"
"buffer_load_dwordx4 v[100:103], v24, %[rwv], s68 offen\n"
"s_mov_b32 s68, 3932160\n"
"buffer_load_dwordx4 v[104:107], v24, %[rwv], s68 offen\n"
"s_mov_b32 s68, 4063232\n"
"buffer_load_dwordx4 v[108:111], v24, %[rwv], s68 offen\n"
"ds_read_b64_tr_b16 a[240:241], v21\n"
"ds_read_b64_tr_b16 a[242:243], v21 offset:576\n"
"ds_read_b64_tr_b16 a[244:245], v21 offset:9344\n"
"ds_read_b64_tr_b16 a[246:247], v21 offset:9920\n"
"ds_read_b64_tr_b16 a[248:249], v21 offset:32\n"
"ds_read_b64_tr_b16 a[250:251], v21 offset:608\n"
"ds_read_b64_tr_b16 a[252:253], v21 offset:9376\n"
"ds_read_b64_tr_b16 a[254:255], v21 offset:9952\n"
"s_waitcnt lgkmcnt(0)\n"
"s_barrier\n"
"s_waitcnt vmcnt(31)\n"
"ds_write_b128 v20, v[128:131]\n"
"s_waitcnt vmcnt(30)\n"
"ds_write_b128 v20, v[132:135] offset:4608\n"
"s_waitcnt vmcnt(29)\n"
"ds_write_b128 v20, v[136:139] offset:9344\n"
"s_waitcnt vmcnt(28)\n"
"ds_write_b128 v20, v[140:143] offset:13952\n"
"s_waitcnt lgkmcnt(0)\n"
"s_barrier\n"
"ds_read_b64_tr_b16 v[128:129], v21\n"
"ds_read_b64_tr_b16 v[130:131], v21 offset:576\n"
"ds_read_b64_tr_b16 v[132:133], v21 offset:9344\n"
"ds_read_b64_tr_b16 v[134:135], v21 offset:9920\n"
"ds_read_b64_tr_b16 v[136:137], v21 offset:32\n"
"ds_read_b64_tr_b16 v[138:139], v21 offset:608\n"
"ds_read_b64_tr_b16 v[140:141], v21 offset:9376\n"
"ds_read_b64_tr_b16 v[142:143], v21 offset:9952\n"
"s_waitcnt lgkmcnt(0)\n"
"s_barrier\n"
"s_waitcnt vmcnt(27)\n"
"ds_write_b128 v20, v[144:147]\n"
"s_waitcnt vmcnt(26)\n"
"ds_write_b128 v20, v[148:151] offset:4608\n"
"s_waitcnt vmcnt(25)\n"
"ds_write_b128 v20, v[152:155] offset:9344\n"
"s_waitcnt vmcnt(24)\n"
"ds_write_b128 v20, v[156:159] offset:13952\n"
"s_waitcnt lgkmcnt(0)\n"
"s_barrier\n"
"ds_read_b64_tr_b16 v[144:145], v21\n"
"ds_read_b64_tr_b16 v[146:147], v21 offset:576\n"
"ds_read_b64_tr_b16 v[148:149], v21 offset:9344\n"
"ds_read_b64_tr_b16 v[150:151], v21 offset:9920\n"
"ds_read_b64_tr_b16 v[152:153], v21 offset:32\n"
"ds_read_b64_tr_b16 v[154:155], v21 offset:608\n"
"ds_read_b64_tr_b16 v[156:157], v21 offset:9376\n"
"ds_read_b64_tr_b16 v[158:159], v21 offset:9952\n"
"s_waitcnt lgkmcnt(0)\n"
"s_barrier\n"
"s_waitcnt vmcnt(23)\n"
"ds_write_b128 v20, v[160:163]\n"
"s_waitcnt vmcnt(22)\n"
"ds_write_b128 v20, v[164:167] offset:4608\n"
"s_waitcnt vmcnt(21)\n"
"ds_write_b128 v20, v[168:171] offset:9344\n"
"s_waitcnt vmcnt(20)\n"
"ds_write_b128 v20, v[172:175] offset:13952\n"
"s_waitcnt lgkmcnt(0)\n"
"s_barrier\n"
"ds_read_b64_tr_b16 v[160:161], v21\n"
"ds_read_b64_tr_b16 v[162:163], v21 offset:576\n"
"ds_read_b64_tr_b16 v[164:165], v21 offset:9344\n"
"ds_read_b64_tr_b16 v[166:167], v21 offset:9920\n"
"ds_read_b64_tr_b16 v[168:169], v21 offset:32\n"
"ds_read_b64_tr_b16 v[170:171], v21 offset:608\n"
"ds_read_b64_tr_b16 v[172:173], v21 offset:9376\n"
"ds_read_b64_tr_b16 v[174:175], v21 offset:9952\n"
"s_waitcnt lgkmcnt(0)\n"
"s_barrier\n"
"s_waitcnt vmcnt(19)\n"
"ds_write_b128 v20, v[176:179]\n"
"s_waitcnt vmcnt(18)\n"
"ds_write_b128 v20, v[180:183] offset:4608\n"
"s_waitcnt vmcnt(17)\n"
"ds_write_b128 v20, v[184:187] offset:9344\n"
"s_waitcnt vmcnt(16)\n"
"ds_write_b128 v20, v[188:191] offset:13952\n"
"s_waitcnt lgkmcnt(0)\n"
"s_barrier\n"
"ds_read_b64_tr_b16 v[176:177], v21\n"
"ds_read_b64_tr_b16 v[178:179], v21 offset:576\n"
"ds_read_b64_tr_b16 v[180:181], v21 offset:9344\n"
"ds_read_b64_tr_b16 v[182:183], v21 offset:9920\n"
"ds_read_b64_tr_b16 v[184:185], v21 offset:32\n"
"ds_read_b64_tr_b16 v[186:187], v21 offset:608\n"
"ds_read_b64_tr_b16 v[188:189], v21 offset:9376\n"
"ds_read_b64_tr_b16 v[190:191], v21 offset:9952\n"
"s_waitcnt lgkmcnt(0)\n"
"s_barrier\n"
"s_waitcnt vmcnt(15)\n"
"ds_write_b128 v20, v[192:195]\n"
"s_waitcnt vmcnt(14)\n"
"ds_write_b128 v20, v[196:199] offset:4608\n"
"s_waitcnt vmcnt(13)\n"
"ds_write_b128 v20, v[200:203] offset:9344\n"
"s_waitcnt vmcnt(12)\n"
"ds_write_b128 v20, v[204:207] offset:13952\n"
"s_waitcnt lgkmcnt(0)\n"
"s_barrier\n"
"ds_read_b64_tr_b16 v[192:193], v21\n"
"ds_read_b64_tr_b16 v[194:195], v21 offset:576\n"
"ds_read_b64_tr_b16 v[196:197], v21 offset:9344\n"
"ds_read_b64_tr_b16 v[198:199], v21 offset:9920\n"
"ds_read_b64_tr_b16 v[200:201], v21 offset:32\n"
"ds_read_b64_tr_b16 v[202:203], v21 offset:608\n"
"ds_read_b64_tr_b16 v[204:205], v21 offset:9376\n"
"ds_read_b64_tr_b16 v[206:207], v21 offset:9952\n"
"s_waitcnt lgkmcnt(0)\n"
"s_barrier\n"
"s_waitcnt vmcnt(11)\n"
"ds_write_b128 v20, v[208:211]\n"
"s_waitcnt vmcnt(10)\n"
"ds_write_b128 v20, v[212:215] offset:4608\n"
"s_waitcnt vmcnt(9)\n"
"ds_write_b128 v20, v[216:219] offset:9344\n"
"s_waitcnt vmcnt(8)\n"
"ds_write_b128 v20, v[220:223] offset:13952\n"
"s_waitcnt lgkmcnt(0)\n"
"s_barrier\n"
"ds_read_b64_tr_b16 v[208:209], v21\n"
"ds_read_b64_tr_b16 v[210:211], v21 offset:576\n"
"ds_read_b64_tr_b16 v[212:213], v21 offset:9344\n"
"ds_read_b64_tr_b16 v[214:215], v21 offset:9920\n"
"ds_read_b64_tr_b16 v[216:217], v21 offset:32\n"
"ds_read_b64_tr_b16 v[218:219], v21 offset:608\n"
"ds_read_b64_tr_b16 v[220:221], v21 offset:9376\n"
"ds_read_b64_tr_b16 v[222:223], v21 offset:9952\n"
"s_waitcnt lgkmcnt(0)\n"
"s_barrier\n"
"s_waitcnt vmcnt(7)\n"
"ds_write_b128 v20, v[224:227]\n"
"s_waitcnt vmcnt(6)\n"
"ds_write_b128 v20, v[228:231] offset:4608\n"
"s_waitcnt vmcnt(5)\n"
"ds_write_b128 v20, v[232:235] offset:9344\n"
"s_waitcnt vmcnt(4)\n"
"ds_write_b128 v20, v[236:239] offset:13952\n"
"s_waitcnt lgkmcnt(0)\n"
"s_barrier\n"
"ds_read_b64_tr_b16 v[224:225], v21\n"
"ds_read_b64_tr_b16 v[226:227], v21 offset:576\n"
"ds_read_b64_tr_b16 v[228:229], v21 offset:9344\n"
"ds_read_b64_tr_b16 v[230:231], v21 offset:9920\n"
"ds_read_b64_tr_b16 v[232:233], v21 offset:32\n"
"ds_read_b64_tr_b16 v[234:235], v21 offset:608\n"
"ds_read_b64_tr_b16 v[236:237], v21 offset:9376\n"
"ds_read_b64_tr_b16 v[238:239], v21 offset:9952\n"
"s_waitcnt lgkmcnt(0)\n"
"s_barrier\n"
"s_waitcnt vmcnt(3)\n"
"ds_write_b128 v20, v[96:99]\n"
"s_waitcnt vmcnt(2)\n"
"ds_write_b128 v20, v[100:103] offset:4608\n"
"s_waitcnt vmcnt(1)\n"
"ds_write_b128 v20, v[104:107] offset:9344\n"
"s_waitcnt vmcnt(0)\n"
"ds_write_b128 v20, v[108:111] offset:13952\n"
"s_waitcnt lgkmcnt(0)\n"
"s_barrier\n"
"ds_read_b64_tr_b16 v[32:33], v21\n"
"ds_read_b64_tr_b16 v[34:35], v21 offset:576\n"
"ds_read_b64_tr_b16 v[36:37], v21 offset:9344\n"
"ds_read_b64_tr_b16 v[38:39], v21 offset:9920\n"
"ds_read_b64_tr_b16 v[40:41], v21 offset:32\n"
"ds_read_b64_tr_b16 v[42:43], v21 offset:608\n"
"ds_read_b64_tr_b16 v[44:45], v21 offset:9376\n"
"ds_read_b64_tr_b16 v[46:47], v21 offset:9952\n"
"s_waitcnt lgkmcnt(0)\n"
"s_barrier\n"
"ds_write_b128 v10, v[32:35] offset:16384\n"
"ds_write_b128 v10, v[36:39] offset:17408\n"
"ds_write_b128 v10, v[40:43] offset:18432\n"
"ds_write_b128 v10, v[44:47] offset:19456\n"
"s_waitcnt lgkmcnt(0)\n"
"s_barrier\n"
"v_mov_b32 v253, 0\n"
"s_mov_b32 s69, 0\n"
"L_par%=:\n"
"buffer_load_dword v252, v253, %[rw], s80 offen sc1\n"
"s_waitcnt vmcnt(0)\n"
"v_readfirstlane_b32 s68, v252\n"
"s_cmp_lg_u32 s68, -1\n"
"s_cbranch_scc1 L_park%=\n"
"s_sleep 8\n"
"s_add_u32 s69, s69, 1\n"
"s_cmp_lt_u32 s69, 0x40000\n"
"s_cbranch_scc1 L_par%=\n"
"L_park%=:\n"
"s_and_b32 s68, s68, 1\n"
"s_lshl_b32 s68, s68, 13\n"
"v_add_u32 v2, s68, v2\n"
"s_add_u32 s87, s87, s68\n"
"s_add_u32 s94, s94, s68\n"
"s_add_u32 s95, s95, s68\n"
"v_mov_b32 v14, 0x3f803f80\n"
"v_mov_b32 v15, 0x3f803f80\n"
"v_mov_b32 v16, 0x3f803f80\n"
"v_mov_b32 v17, 0x3f803f80\n"
"v_mov_b32 v18, 0x3f003f00\n"
"v_mov_b32 v19, 0x3f003f00\n"
"s_mov_b32 s76, 0xff00ff00\n"
"s_mov_b32 s77, 0xff00ff00\n"
"s_mov_b32 s70, 0x3e0293ee\n"
"s_mov_b32 s58, 66\n"
"s_mov_b32 s60, 0\n"
"s_mov_b32 s71, 0\n"
"s_mov_b32 s72, s82\n"
"s_mov_b32 s66, s83\n"
"s_mov_b32 s67, s83\n"
"s_mov_b32 s69, 0\n"
"s_branch L_pre%=\n"
"L_step%=:\n"
"buffer_load_dwordx4 v[20:23], v3, %[rw], s61 offen sc1\n"
"s_nop 11\n"
"buffer_load_dwordx4 v[24:27], v3, %[rw], s61 offen offset:1024 sc1\n"
"s_nop 11\n"
"buffer_load_dwordx4 v[28:31], v3, %[rw], s61 offen offset:2048 sc1\n"
"s_nop 11\n"
"buffer_load_dwordx4 v[32:35], v3, %[rw], s61 offen offset:3072 sc1\n"
"s_nop 11\n"
"buffer_load_dwordx4 v[36:39], v3, %[rw], s74 offen sc1\n"
"s_nop 11\n"
"buffer_load_dwordx4 v[40:43], v3, %[rw], s74 offen offset:1024 sc1\n"
"s_nop 11\n"
"buffer_load_dwordx4 v[44:47], v3, %[rw], s74 offen offset:2048 sc1\n"
"s_nop 11\n"
"buffer_load_dwordx4 v[48:51], v3, %[rw], s74 offen offset:3072 sc1\n"
"v_lshl_add_u32 v12, s91, 8, v7\n"
"ds_read_b64_tr_b16 v[108:109], v12\n"
"ds_read_b64_tr_b16 v[110:111], v12 offset:128\n"
"v_mov_b64 v[240:241], 0\n"
"v_mov_b64 v[242:243], 0\n"
"v_mov_b64 v[244:245], 0\n"
"v_mov_b64 v[246:247], 0\n"
"buffer_store_dwordx4 v[124:127], v0, %[rw], s67 offen sc1\n"
"s_waitcnt lgkmcnt(0)\n"
"buffer_store_dwordx4 v[108:111], v11, %[rw], s66 offen sc1\n"
"s_and_b32 s68, s60, 64\n"
"s_add_u32 s73, s68, 64\n"
"s_lshl_b32 s68, s68, 10\n"
"s_add_u32 s68, s68, s84\n"
"s_cmp_lt_u32 s72, s73\n"
"s_cselect_b32 s68, s68, s83\n"
"v_and_b32 v252, 0xf0, v0\n"
"v_lshlrev_b32 v252, 3, v252\n"
"buffer_load_dword v13, v252, %[rw], s68 offen sc1\n"
"L_comp%=:\n"
"s_waitcnt vmcnt(10)\n"
"v_smfmac_f32_16x16x64_bf16 v[92:95], v[20:23], a[0:7], v1\n"
"v_smfmac_f32_16x16x64_bf16 v[96:99], v[20:23], a[8:15], v1\n"
"v_max3_u32 v252, v20, v21, v22\n"
"v_smfmac_f32_16x16x64_bf16 v[100:103], v[20:23], a[128:135], v1\n"
"v_max_u32 v252, v252, v23\n"
"v_smfmac_f32_16x16x64_bf16 v[104:107], v[20:23], a[136:143], v1\n"
"s_waitcnt vmcnt(9)\n"
"v_smfmac_f32_16x16x64_bf16 v[92:95], v[24:27], a[16:23], v1\n"
"v_max3_u32 v252, v252, v24, v25\n"
"v_smfmac_f32_16x16x64_bf16 v[96:99], v[24:27], a[24:31], v1\n"
"v_max3_u32 v252, v252, v26, v27\n"
"v_smfmac_f32_16x16x64_bf16 v[100:103], v[24:27], a[144:151], v1\n"
"v_smfmac_f32_16x16x64_bf16 v[104:107], v[24:27], a[152:159], v1\n"
"s_waitcnt vmcnt(8)\n"
"v_smfmac_f32_16x16x64_bf16 v[92:95], v[28:31], a[32:39], v1\n"
"v_max3_u32 v252, v252, v28, v29\n"
"v_smfmac_f32_16x16x64_bf16 v[96:99], v[28:31], a[40:47], v1\n"
"v_max3_u32 v252, v252, v30, v31\n"
"v_smfmac_f32_16x16x64_bf16 v[100:103], v[28:31], a[160:167], v1\n"
"v_smfmac_f32_16x16x64_bf16 v[104:107], v[28:31], a[168:175], v1\n"
"s_waitcnt vmcnt(7)\n"
"v_smfmac_f32_16x16x64_bf16 v[92:95], v[32:35], a[48:55], v1\n"
"v_max3_u32 v252, v252, v32, v33\n"
"v_smfmac_f32_16x16x64_bf16 v[96:99], v[32:35], a[56:63], v1\n"
"v_max3_u32 v252, v252, v34, v35\n"
"v_smfmac_f32_16x16x64_bf16 v[100:103], v[32:35], a[176:183], v1\n"
"v_smfmac_f32_16x16x64_bf16 v[104:107], v[32:35], a[184:191], v1\n"
"s_waitcnt vmcnt(6)\n"
"v_smfmac_f32_16x16x64_bf16 v[92:95], v[36:39], a[64:71], v1\n"
"v_max3_u32 v252, v252, v36, v37\n"
"v_smfmac_f32_16x16x64_bf16 v[96:99], v[36:39], a[72:79], v1\n"
"v_max3_u32 v252, v252, v38, v39\n"
"v_smfmac_f32_16x16x64_bf16 v[100:103], v[36:39], a[192:199], v1\n"
"v_smfmac_f32_16x16x64_bf16 v[104:107], v[36:39], a[200:207], v1\n"
"s_memrealtime s[54:55]\n"
"s_waitcnt vmcnt(5)\n"
"v_smfmac_f32_16x16x64_bf16 v[92:95], v[40:43], a[80:87], v1\n"
"v_max3_u32 v252, v252, v40, v41\n"
"v_smfmac_f32_16x16x64_bf16 v[96:99], v[40:43], a[88:95], v1\n"
"v_max3_u32 v252, v252, v42, v43\n"
"v_smfmac_f32_16x16x64_bf16 v[100:103], v[40:43], a[208:215], v1\n"
"v_mov_b32 v113, s60\n"
"v_smfmac_f32_16x16x64_bf16 v[104:107], v[40:43], a[216:223], v1\n"
"s_waitcnt lgkmcnt(0)\n"
"s_add_u32 s68, s54, s58\n"
"v_mov_b32 v112, s68\n"
"s_waitcnt vmcnt(4)\n"
"v_smfmac_f32_16x16x64_bf16 v[92:95], v[44:47], a[96:103], v1\n"
"v_max3_u32 v252, v252, v44, v45\n"
"v_smfmac_f32_16x16x64_bf16 v[96:99], v[44:47], a[104:111], v1\n"
"v_max3_u32 v252, v252, v46, v47\n"
"s_waitcnt vmcnt(3)\n"
"v_smfmac_f32_16x16x64_bf16 v[92:95], v[48:51], a[112:119], v1\n"
"v_max3_u32 v252, v252, v48, v49\n"
"v_smfmac_f32_16x16x64_bf16 v[96:99], v[48:51], a[120:127], v1\n"
"v_max3_u32 v252, v252, v50, v51\n"
"v_smfmac_f32_16x16x64_bf16 v[100:103], v[44:47], a[224:231], v1\n"
"v_cmp_eq_u32 vcc, -1, v252\n"
"s_cbranch_vccnz L_retry%=\n"
"L_ok%=:\n"
"buffer_store_dwordx2 v[112:113], off, %[rw], s95 sc1\n"
"v_smfmac_f32_16x16x64_bf16 v[104:107], v[44:47], a[232:239], v1\n"
"v_add_f32 v92, v92, v94\n"
"v_smfmac_f32_16x16x64_bf16 v[100:103], v[48:51], a[240:247], v1\n"
"v_add_f32 v93, v93, v95\n"
"v_smfmac_f32_16x16x64_bf16 v[104:107], v[48:51], a[248:255], v1\n"
"v_add_f32 v96, v96, v98\n"
"v_add_f32 v97, v97, v99\n"
"v_smfmac_f32_16x16x64_bf16 v[240:243], v[20:23], v[128:135], v1\n"
"v_cvt_pk_bf16_f32 v92, v92, v93\n"
"v_cvt_pk_bf16_f32 v96, v96, v97\n"
"ds_write2_b32 v4, v92, v96 offset1:64\n"
"v_smfmac_f32_16x16x64_bf16 v[244:247], v[20:23], v[136:143], v1\n"
"v_add_f32 v100, v100, v102\n"
"v_add_f32 v101, v101, v103\n"
"v_add_f32 v104, v104, v106\n"
"v_add_f32 v105, v105, v107\n"
"v_cvt_pk_bf16_f32 v102, v100, v101\n"
"v_cvt_pk_bf16_f32 v103, v100, v101\n"
"v_cvt_pk_bf16_f32 v106, v104, v105\n"
"v_cvt_pk_bf16_f32 v107, v104, v105\n"
"ds_write2_b64 v6, v[102:103], v[106:107] offset1:16\n"
"v_smfmac_f32_16x16x64_bf16 v[240:243], v[24:27], v[144:151], v1\n"
"v_smfmac_f32_16x16x64_bf16 v[244:247], v[24:27], v[152:159], v1\n"
"v_smfmac_f32_16x16x64_bf16 v[240:243], v[28:31], v[160:167], v1\n"
"s_and_b32 s68, s60, 63\n"
"s_cmp_eq_u32 s68, 63\n"
"v_smfmac_f32_16x16x64_bf16 v[244:247], v[28:31], v[168:175], v1\n"
"s_waitcnt lgkmcnt(0)\n"
"s_barrier\n"
"s_cbranch_scc1 L_last%=\n"
"ds_read_b64_tr_b16 v[108:109], v5\n"
"ds_read_b64_tr_b16 v[110:111], v5 offset:256\n"
"ds_read_b64_tr_b16 v[112:113], v5 offset:512\n"
"v_smfmac_f32_16x16x64_bf16 v[240:243], v[32:35], v[176:183], v1\n"
"ds_read_b64_tr_b16 v[114:115], v5 offset:768\n"
"ds_read_b64_tr_b16 v[116:117], v5 offset:1024\n"
"v_smfmac_f32_16x16x64_bf16 v[244:247], v[32:35], v[184:191], v1\n"
"ds_read_b64_tr_b16 v[118:119], v5 offset:1280\n"
"ds_read_b64_tr_b16 v[120:121], v5 offset:1536\n"
"v_smfmac_f32_16x16x64_bf16 v[240:243], v[36:39], v[192:199], v1\n"
"ds_read_b64_tr_b16 v[122:123], v5 offset:1792\n"
"s_waitcnt lgkmcnt(6)\n"
"v_mfma_f32_16x16x32_bf16 v[92:95], v[52:55], v[108:111], 0\n"
"ds_read_b64_tr_b16 v[52:53], v7\n"
"ds_read_b64_tr_b16 v[54:55], v7 offset:128\n"
"v_mfma_f32_16x16x32_bf16 v[96:99], v[68:71], v[108:111], 0\n"
"s_waitcnt lgkmcnt(6)\n"
"v_mfma_f32_16x16x32_bf16 v[92:95], v[56:59], v[112:115], v[92:95]\n"
"ds_read_b64_tr_b16 v[56:57], v7 offset:256\n"
"v_mfma_f32_16x16x32_bf16 v[96:99], v[72:75], v[112:115], v[96:99]\n"
"ds_read_b128 v[68:71], v10 offset:16384\n"
"s_waitcnt lgkmcnt(6)\n"
"v_mfma_f32_16x16x32_bf16 v[92:95], v[60:63], v[116:119], v[92:95]\n"
"ds_read_b64_tr_b16 v[58:59], v7 offset:384\n"
"ds_read_b64_tr_b16 v[60:61], v7 offset:512\n"
"v_mfma_f32_16x16x32_bf16 v[96:99], v[76:79], v[116:119], v[96:99]\n"
"ds_read_b128 v[72:75], v10 offset:17408\n"
"s_waitcnt lgkmcnt(7)\n"
"v_mfma_f32_16x16x32_bf16 v[92:95], v[64:67], v[120:123], v[92:95]\n"
"ds_read_b64_tr_b16 v[62:63], v7 offset:640\n"
"ds_read_b64_tr_b16 v[64:65], v7 offset:768\n"
"v_mfma_f32_16x16x32_bf16 v[96:99], v[80:83], v[120:123], v[96:99]\n"
"ds_read_b128 v[76:79], v10 offset:18432\n"
"ds_read_b64_tr_b16 v[66:67], v7 offset:896\n"
"s_waitcnt lgkmcnt(9)\n"
"v_mfma_f32_16x16x32_bf16 v[248:251], v[52:55], v[108:111], 0\n"
"ds_read_b128 v[80:83], v10 offset:19456\n"
"s_waitcnt lgkmcnt(7)\n"
"v_mfma_f32_16x16x32_bf16 v[248:251], v[56:59], v[112:115], v[248:251]\n"
"s_waitcnt lgkmcnt(4)\n"
"v_mfma_f32_16x16x32_bf16 v[248:251], v[60:63], v[116:119], v[248:251]\n"
"v_max3_f32 v252, v92, v93, v94\n"
"v_max3_f32 v253, v95, v96, v97\n"
"s_waitcnt lgkmcnt(1)\n"
"v_mfma_f32_16x16x32_bf16 v[248:251], v[64:67], v[120:123], v[248:251]\n"
"v_max3_f32 v252, v252, v98, v99\n"
"v_smfmac_f32_16x16x64_bf16 v[244:247], v[36:39], v[200:207], v1\n"
"ds_read_b128 v[52:55], v10\n"
"v_smfmac_f32_16x16x64_bf16 v[240:243], v[40:43], v[208:215], v1\n"
"ds_read_b128 v[56:59], v10 offset:1024\n"
"v_mul_f32 v248, s70, v248\n"
"v_mul_f32 v249, s70, v249\n"
"v_max3_f32 v253, v253, v248, v249\n"
"v_max_f32 v252, v252, v253\n"
"v_mov_b32 v253, v252\n"
"v_smfmac_f32_16x16x64_bf16 v[244:247], v[40:43], v[216:223], v1\n"
"ds_read_b128 v[60:63], v10 offset:4096\n"
"v_permlane16_swap_b32 v252, v253\n"
"v_max_f32 v252, v252, v253\n"
"v_mov_b32 v253, v252\n"
"v_smfmac_f32_16x16x64_bf16 v[240:243], v[44:47], v[224:231], v1\n"
"ds_read_b128 v[64:67], v10 offset:5120\n"
"v_permlane32_swap_b32 v252, v253\n"
"v_max_f32 v252, v252, v253\n"
"v_smfmac_f32_16x16x64_bf16 v[244:247], v[44:47], v[232:239], v1\n"
"v_sub_f32 v92, v92, v252\n"
"v_smfmac_f32_16x16x64_bf16 v[240:243], v[48:51], v[68:75], v1\n"
"v_sub_f32 v93, v93, v252\n"
"s_waitcnt lgkmcnt(4)\n"
"v_smfmac_f32_16x16x64_bf16 v[244:247], v[48:51], v[76:83], v1\n"
"v_sub_f32 v94, v94, v252\n"
"v_exp_f32 v92, v92\n"
"ds_read_b128 v[112:115], v10 offset:2048\n"
"v_exp_f32 v93, v93\n"
"v_sub_f32 v95, v95, v252\n"
"v_exp_f32 v94, v94\n"
"ds_read_b128 v[116:119], v10 offset:3072\n"
"v_sub_f32 v96, v96, v252\n"
"v_exp_f32 v95, v95\n"
"v_sub_f32 v97, v97, v252\n"
"ds_read_b128 v[20:23], v10 offset:6144\n"
"v_exp_f32 v96, v96\n"
"v_sub_f32 v98, v98, v252\n"
"v_exp_f32 v97, v97\n"
"ds_read_b128 v[24:27], v10 offset:7168\n"
"v_sub_f32 v99, v99, v252\n"
"v_exp_f32 v98, v98\n"
"v_sub_f32 v248, v248, v252\n"
"ds_read_b128 v[28:31], v10 offset:8192\n"
"v_exp_f32 v99, v99\n"
"v_sub_f32 v249, v249, v252\n"
"v_exp_f32 v248, v248\n"
"ds_read_b128 v[32:35], v10 offset:9216\n"
"v_exp_f32 v249, v249\n"
"v_cvt_pk_bf16_f32 v100, v92, v93\n"
"v_cvt_pk_bf16_f32 v101, v94, v95\n"
"ds_read_b128 v[36:39], v10 offset:12288\n"
"v_cvt_pk_bf16_f32 v102, v96, v97\n"
"v_cvt_pk_bf16_f32 v103, v98, v99\n"
"v_cvt_pk_bf16_f32 v104, v248, v249\n"
"v_cvt_pk_bf16_f32 v105, v248, v249\n"
"v_mfma_f32_16x16x32_bf16 v[108:111], v[14:17], v[100:103], 0\n"
"v_cvt_pk_bf16_f32 v124, v240, v241\n"
"v_cvt_pk_bf16_f32 v125, v242, v243\n"
"v_mfma_f32_16x16x32_bf16 v[92:95], v[84:87], v[100:103], 0\n"
"v_cvt_pk_bf16_f32 v126, v244, v245\n"
"v_cvt_pk_bf16_f32 v127, v246, v247\n"
"v_mfma_f32_16x16x32_bf16 v[96:99], v[88:91], v[100:103], 0\n"
"ds_read_b128 v[40:43], v10 offset:13312\n"
"v_mfma_f32_16x16x16_bf16 v[108:111], v[18:19], v[104:105], v[108:111]\n"
"v_mfma_f32_16x16x16_bf16 v[92:95], v[124:125], v[104:105], v[92:95]\n"
"ds_read_b128 v[84:87], v10 offset:10240\n"
"v_mfma_f32_16x16x16_bf16 v[96:99], v[126:127], v[104:105], v[96:99]\n"
"v_mov_b64 v[240:241], 0\n"
"v_mov_b64 v[242:243], 0\n"
"v_rcp_f32 v252, v108\n"
"ds_read_b128 v[88:91], v10 offset:11264\n"
"v_mov_b64 v[244:245], 0\n"
"v_mov_b64 v[246:247], 0\n"
"s_nop 1\n"
"v_cndmask_b32 v92, v92, v96, s[76:77]\n"
"v_cndmask_b32 v93, v93, v97, s[76:77]\n"
"v_cndmask_b32 v94, v94, v98, s[76:77]\n"
"v_cndmask_b32 v95, v95, v99, s[76:77]\n"
"v_mul_f32 v92, v92, v252\n"
"v_mul_f32 v93, v93, v252\n"
"v_mul_f32 v94, v94, v252\n"
"v_mul_f32 v95, v95, v252\n"
"v_cvt_pk_bf16_f32 v92, v92, v93\n"
"v_cvt_pk_bf16_f32 v93, v94, v95\n"
"ds_write_b64 v8, v[92:93]\n"
"v_mov_b64 v[248:249], 0\n"
"v_mov_b64 v[250:251], 0\n"
"v_mov_b64 v[100:101], 0\n"
"v_mov_b64 v[102:103], 0\n"
"s_waitcnt lgkmcnt(0)\n"
"s_barrier\n"
"ds_read_b128 v[92:95], v9\n"
"ds_read_b128 v[96:99], v9 offset:128\n"
"ds_read_b128 v[44:47], v10 offset:14336\n"
"ds_read_b128 v[48:51], v10 offset:15360\n"
"v_lshlrev_b32 v12, 3, v0\n"
"buffer_load_dwordx2 v[120:121], v12, %[rw], s94 offen sc1\n"
"s_waitcnt lgkmcnt(3)\n"
"v_smfmac_f32_16x16x64_bf16 v[240:243], v[92:95], v[52:59], v1\n"
"v_smfmac_f32_16x16x64_bf16 v[244:247], v[92:95], v[60:67], v1\n"
"s_waitcnt lgkmcnt(2)\n"
"v_smfmac_f32_16x16x64_bf16 v[240:243], v[96:99], v[112:119], v1\n"
"v_smfmac_f32_16x16x64_bf16 v[244:247], v[96:99], v[20:27], v1\n"
"v_smfmac_f32_16x16x64_bf16 v[248:251], v[92:95], v[28:35], v1\n"
"v_smfmac_f32_16x16x64_bf16 v[100:103], v[92:95], v[36:43], v1\n"
"v_add_f32 v240, v240, v242\n"
"v_smfmac_f32_16x16x64_bf16 v[248:251], v[96:99], v[84:91], v1\n"
"v_add_f32 v241, v241, v243\n"
"s_waitcnt lgkmcnt(0)\n"
"v_smfmac_f32_16x16x64_bf16 v[100:103], v[96:99], v[44:51], v1\n"
"v_cvt_pk_bf16_f32 v104, v240, v241\n"
"v_add_f32 v244, v244, v246\n"
"v_add_f32 v245, v245, v247\n"
"v_cvt_pk_bf16_f32 v105, v244, v245\n"
"v_add_f32 v248, v248, v250\n"
"v_add_f32 v249, v249, v251\n"
"v_cvt_pk_bf16_f32 v106, v248, v249\n"
"s_nop 0\n"
"v_add_f32 v100, v100, v102\n"
"v_add_f32 v101, v101, v103\n"
"v_cvt_pk_bf16_f32 v107, v100, v101\n"
"buffer_store_dwordx4 v[104:107], v2, %[rw], s62 offen sc1\n"
"s_memrealtime s[56:57]\n"
"s_waitcnt lgkmcnt(0)\n"
"s_sub_u32 s68, s56, s54\n"
"s_add_u32 s58, s58, 2\n"
"s_min_u32 s58, s58, s68\n"
"s_sub_u32 s68, s60, 1\n"
"s_and_b32 s73, s60, 64\n"
"s_max_i32 s68, s68, s73\n"
"v_mov_b32 v253, s68\n"
"buffer_store_dword v253, off, %[rw], s90 sc1\n"
"L_next%=:\n"
"s_lshr_b32 s68, s60, 6\n"
"s_mul_i32 s68, s68, 0x4c00000\n"
"s_and_b32 s73, s60, 63\n"
"s_lshl_b32 s66, s73, 16\n"
"s_add_u32 s66, s66, s68\n"
"s_add_u32 s66, s66, s88\n"
"s_lshl_b32 s67, s73, 17\n"
"s_add_u32 s67, s67, s68\n"
"s_add_u32 s67, s67, s89\n"
"s_cmp_eq_u32 s93, 0\n"
"s_cselect_b32 s66, s83, s66\n"
"s_cselect_b32 s67, s83, s67\n"
"s_cmp_eq_u32 s73, 63\n"
"s_cbranch_scc1 L_fin%=\n"
"s_waitcnt vmcnt(4)\n"
"s_waitcnt vmcnt(4)\n"
"v_add_u32 v253, 1, v13\n"
"v_lshlrev_b32 v253, 2, v253\n"
"v_bfe_u32 v252, v0, 4, 2\n"
"v_add_u32 v253, v253, v252\n"
"s_nop 1\n"
"v_min_u32_dpp v253, v253, v253 row_ror:8 row_mask:0xf bank_mask:0xf\n"
"s_nop 1\n"
"v_min_u32_dpp v253, v253, v253 row_ror:4 row_mask:0xf bank_mask:0xf\n"
"s_nop 1\n"
"v_min_u32_dpp v253, v253, v253 row_ror:2 row_mask:0xf bank_mask:0xf\n"
"s_nop 1\n"
"v_min_u32_dpp v253, v253, v253 row_ror:1 row_mask:0xf bank_mask:0xf\n"
"s_nop 0\n"
"v_readlane_b32 s68, v253, 0\n"
"s_min_u32 s68, s68, 64\n"
"s_and_b32 s73, s60, 64\n"
"s_add_u32 s68, s68, s73\n"
"s_max_u32 s72, s72, s68\n"
"s_add_u32 s60, s60, 1\n"
"L_pre%=:\n"
"s_nop 7\n"
"s_cmp_lt_u32 s60, s72\n"
"s_cbranch_scc1 L_rdy%=\n"
"L_rpoll%=:\n"
"s_and_b32 s68, s60, 64\n"
"s_lshl_b32 s68, s68, 10\n"
"s_add_u32 s68, s68, s84\n"
"v_and_b32 v252, 0xf0, v0\n"
"v_lshlrev_b32 v252, 3, v252\n"
"buffer_load_dword v253, v252, %[rw], s68 offen sc1\n"
"s_waitcnt vmcnt(0)\n"
"v_add_u32 v253, 1, v253\n"
"v_lshlrev_b32 v253, 2, v253\n"
"v_lshrrev_b32 v252, 7, v252\n"
"v_and_b32 v252, 3, v252\n"
"v_add_u32 v253, v253, v252\n"
"s_and_b32 s68, s60, 63\n"
"v_cmp_ge_u32 vcc, s68, v253\n"
"s_cbranch_vccz L_rok%=\n"
"s_sleep 4\n"
"s_add_u32 s69, s69, 1\n"
"s_cmp_lt_u32 s69, 0x100000\n"
"s_cbranch_scc1 L_rpoll%=\n"
"s_mov_b32 s72, 1000\n"
"L_rok%=:\n"
"v_min_u32_dpp v253, v253, v253 row_ror:8 row_mask:0xf bank_mask:0xf\n"
"s_nop 1\n"
"v_min_u32_dpp v253, v253, v253 row_ror:4 row_mask:0xf bank_mask:0xf\n"
"s_nop 1\n"
"v_min_u32_dpp v253, v253, v253 row_ror:2 row_mask:0xf bank_mask:0xf\n"
"s_nop 1\n"
"v_min_u32_dpp v253, v253, v253 row_ror:1 row_mask:0xf bank_mask:0xf\n"
"s_nop 0\n"
"v_readlane_b32 s68, v253, 0\n"
"s_min_u32 s68, s68, 64\n"
"s_and_b32 s73, s60, 64\n"
"s_add_u32 s68, s68, s73\n"
"s_max_u32 s72, s72, s68\n"
"L_rdy%=:\n"
"s_lshr_b32 s68, s60, 6\n"
"s_mul_i32 s68, s68, 0x4c00000\n"
"s_and_b32 s73, s60, 63\n"
"s_lshl_b32 s73, s73, 18\n"
"s_add_u32 s68, s68, s73\n"
"s_add_u32 s63, s68, s85\n"
"s_add_u32 s64, s63, 4096\n"
"s_add_u32 s65, s68, s86\n"
"buffer_load_dwordx4 v[52:55], v0, %[rw], s63 offen sc1\n"
"buffer_load_dwordx4 v[56:59], v0, %[rw], s63 offen offset:1024 sc1\n"
"buffer_load_dwordx4 v[60:63], v0, %[rw], s63 offen offset:2048 sc1\n"
"buffer_load_dwordx4 v[64:67], v0, %[rw], s63 offen offset:3072 sc1\n"
"buffer_load_dwordx4 v[68:71], v0, %[rw], s64 offen sc1\n"
"buffer_load_dwordx4 v[72:75], v0, %[rw], s64 offen offset:1024 sc1\n"
"buffer_load_dwordx4 v[76:79], v0, %[rw], s64 offen offset:2048 sc1\n"
"buffer_load_dwordx4 v[80:83], v0, %[rw], s64 offen offset:3072 sc1\n"
"buffer_load_dwordx4 v[84:87], v0, %[rw], s65 offen sc1\n"
"buffer_load_dwordx4 v[88:91], v0, %[rw], s65 offen offset:1024 sc1\n"
"s_and_b32 s68, s60, 3\n"
"s_lshl_b32 s68, s68, 14\n"
"s_add_u32 s61, s68, s87\n"
"s_add_u32 s74, s61, 4096\n"
"s_and_b32 s68, s60, 1\n"
"s_lshl_b32 s62, s68, 19\n"
"v_mov_b64 v[92:93], 0\n"
"v_mov_b64 v[94:95], 0\n"
"v_mov_b64 v[96:97], 0\n"
"v_mov_b64 v[98:99], 0\n"
"v_mov_b64 v[100:101], 0\n"
"v_mov_b64 v[102:103], 0\n"
"v_mov_b64 v[104:105], 0\n"
"v_mov_b64 v[106:107], 0\n"
"s_and_b32 s68, s60, 63\n"
"s_cmp_eq_u32 s68, 0\n"
"s_cselect_b32 s68, 1, s91\n"
"s_cmp_lg_u32 s68, 0\n"
"s_cbranch_scc1 L_go%=\n"
"s_sub_u32 s73, s60, 1\n"
"s_mov_b32 s69, s71\n"
"s_memrealtime s[78:79]\n"
"s_waitcnt vmcnt(12)\n"
"s_branch L_hch%=\n"
"L_hph%=:\n"
"buffer_load_dwordx2 v[120:121], v12, %[rw], s94 offen sc1\n"
"s_memrealtime s[78:79]\n"
"s_waitcnt vmcnt(0)\n"
"L_hch%=:\n"
"v_cmp_gt_i32 vcc, s73, v121\n"
"s_cbranch_vccz L_hkh%=\n"
"s_add_u32 s69, s69, 1\n"
"s_cmp_lt_u32 s69, 0x40000\n"
"s_cbranch_scc1 L_hph%=\n"
"L_hkh%=:\n"
"s_waitcnt lgkmcnt(0)\n"
"v_subrev_u32 v252, s78, v120\n"
"s_nop 1\n"
"v_max_i32_dpp v252, v252, v252 row_ror:8 row_mask:0xf bank_mask:0xf\n"
"s_nop 1\n"
"v_max_i32_dpp v252, v252, v252 row_ror:4 row_mask:0xf bank_mask:0xf\n"
"s_nop 1\n"
"v_max_i32_dpp v252, v252, v252 row_ror:2 row_mask:0xf bank_mask:0xf\n"
"s_nop 1\n"
"v_max_i32_dpp v252, v252, v252 row_ror:1 row_mask:0xf bank_mask:0xf\n"
"s_nop 0\n"
"v_readlane_b32 s59, v252, 0\n"
"v_readlane_b32 s68, v252, 16\n"
"s_max_i32 s59, s59, s68\n"
"v_readlane_b32 s68, v252, 32\n"
"s_max_i32 s59, s59, s68\n"
"v_readlane_b32 s68, v252, 48\n"
"s_max_i32 s59, s59, s68\n"
"s_add_u32 s59, s59, s78\n"
"s_cmp_lt_u32 s60, 84\n"
"s_cselect_b32 s68, 54, 54\n"
"s_add_u32 s59, s59, s68\n"
"L_twh%=:\n"
"s_memrealtime s[78:79]\n"
"s_waitcnt lgkmcnt(0)\n"
"s_sub_u32 s68, s59, s78\n"
"s_cmp_gt_i32 s68, 2000\n"
"s_cselect_b32 s68, 0, s68\n"
"s_cmp_gt_i32 s68, 5\n"
"s_cbranch_scc1 L_twh%=\n"
"s_sub_u32 s68, 5, s68\n"
"s_add_u32 s68, s68, 2\n"
"s_min_i32 s68, s68, 5\n"
"s_lshl_b32 s68, s68, 2\n"
"s_getpc_b64 s[56:57]\n"
"s_add_u32 s68, s68, 16\n"
"s_add_u32 s56, s56, s68\n"
"s_addc_u32 s57, s57, 0\n"
"s_setpc_b64 s[56:57]\n"
"s_nop 5\n"
"s_nop 5\n"
"s_nop 5\n"
"s_nop 5\n"
"s_nop 5\n"
"L_go%=:\n"
"s_barrier\n"
"s_branch L_step%=\n"
"L_retry%=:\n"
"s_cmp_lg_u32 s71, 0\n"
"s_cbranch_scc1 L_ok%=\n"
"s_mov_b32 s69, 0\n"
"s_waitcnt lgkmcnt(0)\n"
"s_nop 9\n"
"v_mov_b64 v[92:93], 0\n"
"v_mov_b64 v[94:95], 0\n"
"v_mov_b64 v[96:97], 0\n"
"v_mov_b64 v[98:99], 0\n"
"v_mov_b64 v[100:101], 0\n"
"v_mov_b64 v[102:103], 0\n"
"v_mov_b64 v[104:105], 0\n"
"v_mov_b64 v[106:107], 0\n"
"L_poll%=:\n"
"buffer_load_dwordx4 v[20:23], v3, %[rw], s61 offen sc1\n"
"buffer_load_dwordx4 v[24:27], v3, %[rw], s61 offen offset:1024 sc1\n"
"buffer_load_dwordx4 v[28:31], v3, %[rw], s61 offen offset:2048 sc1\n"
"buffer_load_dwordx4 v[32:35], v3, %[rw], s61 offen offset:3072 sc1\n"
"buffer_load_dwordx4 v[36:39], v3, %[rw], s74 offen sc1\n"
"buffer_load_dwordx4 v[40:43], v3, %[rw], s74 offen offset:1024 sc1\n"
"buffer_load_dwordx4 v[44:47], v3, %[rw], s74 offen offset:2048 sc1\n"
"buffer_load_dwordx4 v[48:51], v3, %[rw], s74 offen offset:3072 sc1\n"
"s_waitcnt vmcnt(7)\n"
"v_max3_u32 v252, v20, v21, v22\n"
"v_max_u32 v252, v252, v23\n"
"s_waitcnt vmcnt(6)\n"
"v_max3_u32 v252, v252, v24, v25\n"
"v_max3_u32 v252, v252, v26, v27\n"
"s_waitcnt vmcnt(5)\n"
"v_max3_u32 v252, v252, v28, v29\n"
"v_max3_u32 v252, v252, v30, v31\n"
"s_waitcnt vmcnt(4)\n"
"v_max3_u32 v252, v252, v32, v33\n"
"v_max3_u32 v252, v252, v34, v35\n"
"s_waitcnt vmcnt(3)\n"
"v_max3_u32 v252, v252, v36, v37\n"
"v_max3_u32 v252, v252, v38, v39\n"
"s_waitcnt vmcnt(2)\n"
"v_max3_u32 v252, v252, v40, v41\n"
"v_max3_u32 v252, v252, v42, v43\n"
"s_waitcnt vmcnt(1)\n"
"v_max3_u32 v252, v252, v44, v45\n"
"v_max3_u32 v252, v252, v46, v47\n"
"s_waitcnt vmcnt(0)\n"
"v_max3_u32 v252, v252, v48, v49\n"
"v_max3_u32 v252, v252, v50, v51\n"
"v_cmp_eq_u32 vcc, -1, v252\n"
"s_cbranch_vccz L_comp%=\n"
"s_add_u32 s69, s69, 1\n"
"s_cmp_lt_u32 s69, 0x20000\n"
"s_cbranch_scc1 L_poll%=\n"
"s_mov_b32 s71, 0x40000\n"
"s_branch L_comp%=\n"
"L_last%=:\n"
"ds_read_b128 v[68:71], v10 offset:16384\n"
"ds_read_b128 v[72:75], v10 offset:17408\n"
"ds_read_b128 v[76:79], v10 offset:18432\n"
"ds_read_b128 v[80:83], v10 offset:19456\n"
"s_sub_u32 s68, s60, 1\n"
"v_mov_b32 v253, s68\n"
"buffer_store_dword v253, off, %[rw], s90 sc1\n"
"v_smfmac_f32_16x16x64_bf16 v[240:243], v[32:35], v[176:183], v1\n"
"v_smfmac_f32_16x16x64_bf16 v[244:247], v[32:35], v[184:191], v1\n"
"v_smfmac_f32_16x16x64_bf16 v[240:243], v[36:39], v[192:199], v1\n"
"v_smfmac_f32_16x16x64_bf16 v[244:247], v[36:39], v[200:207], v1\n"
"v_smfmac_f32_16x16x64_bf16 v[240:243], v[40:43], v[208:215], v1\n"
"v_smfmac_f32_16x16x64_bf16 v[244:247], v[40:43], v[216:223], v1\n"
"v_smfmac_f32_16x16x64_bf16 v[240:243], v[44:47], v[224:231], v1\n"
"v_smfmac_f32_16x16x64_bf16 v[244:247], v[44:47], v[232:239], v1\n"
"s_waitcnt lgkmcnt(2)\n"
"v_smfmac_f32_16x16x64_bf16 v[240:243], v[48:51], v[68:75], v1\n"
"s_waitcnt lgkmcnt(0)\n"
"v_smfmac_f32_16x16x64_bf16 v[244:247], v[48:51], v[76:83], v1\n"
"s_nop 3\n"
"v_cvt_pk_bf16_f32 v124, v240, v241\n"
"v_cvt_pk_bf16_f32 v125, v242, v243\n"
"s_nop 1\n"
"v_cvt_pk_bf16_f32 v126, v244, v245\n"
"v_cvt_pk_bf16_f32 v127, v246, v247\n"
"s_branch L_next%=\n"
"L_fin%=:\n"
"s_nop 7\n"
"v_lshl_add_u32 v12, s91, 8, v7\n"
"ds_read_b64_tr_b16 v[108:109], v12\n"
"ds_read_b64_tr_b16 v[110:111], v12 offset:128\n"
"buffer_store_dwordx4 v[124:127], v0, %[rw], s67 offen sc1\n"
"s_waitcnt lgkmcnt(0)\n"
"buffer_store_dwordx4 v[108:111], v11, %[rw], s66 offen sc1\n"
"s_waitcnt vmcnt(2)\n"
"s_barrier\n"
"v_mov_b32 v252, s60\n"
"buffer_store_dword v252, off, %[rw], s90 sc1\n"
"s_waitcnt vmcnt(0)\n"
"s_barrier\n"
"s_add_u32 s60, s60, 1\n"
"v_mov_b32 v252, s60\n"
"buffer_store_dword v252, off, %[rw], s90 sc1\n"
"s_mov_b32 s66, s83\n"
"s_mov_b32 s67, s83\n"
"s_mov_b32 s69, 0\n"
"s_cmp_lt_u32 s60, s81\n"
"s_cbranch_scc1 L_pre%=\n"
"s_waitcnt vmcnt(0)\n"
    : "+{v[20:27]}"(c2), "+{v[0:7]}"(c0), "+{s[80:95]}"(sc)
    : "{v[8:11]}"(c1), [rw] "s"(rw), [rwq] "s"(rq), [rwk] "s"(rk), [rwv] "s"(rv), [rwo] "s"(ro)
    : "memory", "vcc", "scc", CLOB_H);
}
DI void reducer_role(const Params& p, int r) {
  const int l = fresh_lane(), g = l >> 4, i16 = l & 15;
  u32x8 c0; u32x8 sc;
  c0[0] = cphys(C_RING + r * 8192) + l * 16;
  c0[1] = i16 < 8 ? cphys(C_MEM) + r * 256 + i16 * 32 + g * 8 : OOB;
#pragma unroll
  for (int i = 0; i < 4; ++i) c0[2 + i] = (i16 == 2 * i ? 0x3f80u : 0u) | (i16 == 2 * i + 1 ? 0x3f800000u : 0u);
  const u32x2 m = *(const GAS u32x2*)(p.mem0 + (2 * (r & 3) + (i16 & 1)) * 512 + 64 * (r >> 2) + 16 * ((i16 & 7) >> 1) + 4 * g);
  c0[6] = nosent(m[0]); c0[7] = nosent(m[1]);
  sc[0] = F_PAR; sc[1] = 128; sc[2] = 0; sc[3] = cphys(C_HINT); sc[4] = 0; sc[5] = 0; sc[6] = 0; sc[7] = 0;
  const rsrc_t rw = mkrsrc(p.ws, W_END);
  asm volatile(
"v_mov_b32 v44, -1\n"
"v_mov_b32 v45, -1\n"
"v_mov_b32 v46, -1\n"
"v_mov_b32 v47, -1\n"
"v_mbcnt_lo_u32_b32 v52, -1, 0\n"
"v_mbcnt_hi_u32_b32 v52, -1, v52\n"
"v_lshlrev_b32 v52, 7, v52\n"
"s_memrealtime s[78:79]\n"
"s_waitcnt lgkmcnt(0)\n"
"s_mov_b32 s67, s78\n"
"v_mov_b32 v49, 0\n"
"s_mov_b32 s64, 0\n"
"L_par%=:\n"
"buffer_load_dword v48, v49, %[rw], s80 offen sc1\n"
"s_waitcnt vmcnt(0)\n"
"v_readfirstlane_b32 s63, v48\n"
"s_cmp_lg_u32 s63, -1\n"
"s_cbranch_scc1 L_park%=\n"
"s_sleep 8\n"
"s_add_u32 s64, s64, 1\n"
"s_cmp_lt_u32 s64, 0x40000\n"
"s_cbranch_scc1 L_par%=\n"
"L_park%=:\n"
"s_and_b32 s63, s63, 1\n"
"s_lshl_b32 s63, s63, 13\n"
"v_add_u32 v0, s63, v0\n"
"v_add_u32 v1, s63, v1\n"
"s_add_u32 s83, s83, s63\n"
"s_mov_b32 s60, 0\n"
"s_mov_b32 s65, 0\n"
"L_layer%=:\n"
"s_waitcnt vmcnt(0)\n"
"buffer_store_dwordx2 v[6:7], v1, %[rw], 0 offen sc1\n"
"s_mov_b32 s63, 0x8000\n"
"buffer_store_dwordx2 v[44:45], v1, %[rw], s63 offen sc1\n"
"L_step%=:\n"
"s_and_b32 s63, s60, 1\n"
"s_lshl_b32 s61, s63, 19\n"
"s_add_u32 s66, s61, 4096\n"
"s_add_u32 s63, s60, 1\n"
"s_and_b32 s63, s63, 3\n"
"s_lshl_b32 s62, s63, 14\n"
"s_mov_b32 s64, s65\n"
"L_twa%=:\n"
"s_memrealtime s[78:79]\n"
"s_waitcnt lgkmcnt(0)\n"
"s_sub_u32 s63, s67, s78\n"
"s_cmp_gt_i32 s63, 2000\n"
"s_cselect_b32 s63, 0, s63\n"
"s_cmp_gt_i32 s63, 5\n"
"s_cbranch_scc1 L_twa%=\n"
"s_sub_u32 s63, 5, s63\n"
"s_add_u32 s63, s63, 2\n"
"s_min_i32 s63, s63, 5\n"
"s_lshl_b32 s63, s63, 2\n"
"s_getpc_b64 s[76:77]\n"
"s_add_u32 s63, s63, 16\n"
"s_add_u32 s76, s76, s63\n"
"s_addc_u32 s77, s77, 0\n"
"s_setpc_b64 s[76:77]\n"
"s_nop 5\n"
"s_nop 5\n"
"s_nop 5\n"
"s_nop 5\n"
"s_nop 5\n"
"L_hpr%=:\n"
"buffer_load_dwordx2 v[50:51], v52, %[rw], s83 offen sc1\n"
"s_memrealtime s[78:79]\n"
"s_waitcnt vmcnt(0)\n"
"L_hcr%=:\n"
"v_cmp_gt_i32 vcc, s60, v51\n"
"s_cbranch_vccz L_hkr%=\n"
"s_add_u32 s64, s64, 1\n"
"s_cmp_lt_u32 s64, 0x40000\n"
"s_cbranch_scc1 L_hpr%=\n"
"L_hkr%=:\n"
"s_waitcnt lgkmcnt(0)\n"
"v_subrev_u32 v48, s78, v50\n"
"s_nop 1\n"
"v_max_i32_dpp v48, v48, v48 row_ror:8 row_mask:0xf bank_mask:0xf\n"
"s_nop 1\n"
"v_max_i32_dpp v48, v48, v48 row_ror:4 row_mask:0xf bank_mask:0xf\n"
"s_nop 1\n"
"v_max_i32_dpp v48, v48, v48 row_ror:2 row_mask:0xf bank_mask:0xf\n"
"s_nop 1\n"
"v_max_i32_dpp v48, v48, v48 row_ror:1 row_mask:0xf bank_mask:0xf\n"
"s_nop 0\n"
"v_readlane_b32 s67, v48, 0\n"
"v_readlane_b32 s63, v48, 16\n"
"s_max_i32 s67, s67, s63\n"
"v_readlane_b32 s63, v48, 32\n"
"s_max_i32 s67, s67, s63\n"
"v_readlane_b32 s63, v48, 48\n"
"s_max_i32 s67, s67, s63\n"
"s_add_u32 s67, s67, s78\n"
"s_cmp_lt_u32 s60, 83\n"
"s_cselect_b32 s63, 8, 8\n"
"s_add_u32 s67, s67, s63\n"
"L_twb%=:\n"
"s_memrealtime s[78:79]\n"
"s_waitcnt lgkmcnt(0)\n"
"s_sub_u32 s63, s67, s78\n"
"s_cmp_gt_i32 s63, 2000\n"
"s_cselect_b32 s63, 0, s63\n"
"s_cmp_gt_i32 s63, 5\n"
"s_cbranch_scc1 L_twb%=\n"
"s_sub_u32 s63, 5, s63\n"
"s_add_u32 s63, s63, 2\n"
"s_min_i32 s63, s63, 5\n"
"s_lshl_b32 s63, s63, 2\n"
"s_getpc_b64 s[76:77]\n"
"s_add_u32 s63, s63, 16\n"
"s_add_u32 s76, s76, s63\n"
"s_addc_u32 s77, s77, 0\n"
"s_setpc_b64 s[76:77]\n"
"s_nop 5\n"
"s_nop 5\n"
"s_nop 5\n"
"s_nop 5\n"
"s_nop 5\n"
"s_add_u32 s67, s67, 110\n"
"L_poll%=:\n"
"buffer_load_dwordx4 v[8:11], v0, %[rw], s61 offen sc1\n"
"buffer_load_dwordx4 v[12:15], v0, %[rw], s61 offen offset:1024 sc1\n"
"buffer_load_dwordx4 v[16:19], v0, %[rw], s61 offen offset:2048 sc1\n"
"buffer_load_dwordx4 v[20:23], v0, %[rw], s61 offen offset:3072 sc1\n"
"buffer_load_dwordx4 v[24:27], v0, %[rw], s66 offen sc1\n"
"buffer_load_dwordx4 v[28:31], v0, %[rw], s66 offen offset:1024 sc1\n"
"buffer_load_dwordx4 v[32:35], v0, %[rw], s66 offen offset:2048 sc1\n"
"buffer_load_dwordx4 v[36:39], v0, %[rw], s66 offen offset:3072 sc1\n"
"s_waitcnt vmcnt(7)\n"
"v_mfma_f32_16x16x32_bf16 v[40:43], v[8:11], v[2:5], 0\n"
"v_max3_u32 v48, v8, v9, v10\n"
"v_max_u32 v48, v48, v11\n"
"s_waitcnt vmcnt(6)\n"
"v_mfma_f32_16x16x32_bf16 v[40:43], v[12:15], v[2:5], v[40:43]\n"
"v_max3_u32 v48, v48, v12, v13\n"
"v_max3_u32 v48, v48, v14, v15\n"
"s_waitcnt vmcnt(5)\n"
"v_mfma_f32_16x16x32_bf16 v[40:43], v[16:19], v[2:5], v[40:43]\n"
"v_max3_u32 v48, v48, v16, v17\n"
"v_max3_u32 v48, v48, v18, v19\n"
"s_waitcnt vmcnt(4)\n"
"v_mfma_f32_16x16x32_bf16 v[40:43], v[20:23], v[2:5], v[40:43]\n"
"v_max3_u32 v48, v48, v20, v21\n"
"v_max3_u32 v48, v48, v22, v23\n"
"s_waitcnt vmcnt(3)\n"
"v_mfma_f32_16x16x32_bf16 v[40:43], v[24:27], v[2:5], v[40:43]\n"
"v_max3_u32 v48, v48, v24, v25\n"
"v_max3_u32 v48, v48, v26, v27\n"
"s_waitcnt vmcnt(2)\n"
"v_mfma_f32_16x16x32_bf16 v[40:43], v[28:31], v[2:5], v[40:43]\n"
"v_max3_u32 v48, v48, v28, v29\n"
"v_max3_u32 v48, v48, v30, v31\n"
"s_waitcnt vmcnt(1)\n"
"v_mfma_f32_16x16x32_bf16 v[40:43], v[32:35], v[2:5], v[40:43]\n"
"v_max3_u32 v48, v48, v32, v33\n"
"v_max3_u32 v48, v48, v34, v35\n"
"s_waitcnt vmcnt(0)\n"
"v_mfma_f32_16x16x32_bf16 v[40:43], v[36:39], v[2:5], v[40:43]\n"
"v_max3_u32 v48, v48, v36, v37\n"
"v_max3_u32 v48, v48, v38, v39\n"
"v_cmp_eq_u32 vcc, -1, v48\n"
"s_cbranch_vccnz L_retry%=\n"
"L_ok%=:\n"
"s_nop 3\n"
"v_cvt_pk_bf16_f32 v40, v40, v41\n"
"v_cvt_pk_bf16_f32 v41, v42, v43\n"
"buffer_store_dwordx2 v[40:41], v1, %[rw], s62 offen sc1\n"
"s_sleep 12\n"
"buffer_store_dwordx4 v[44:47], v0, %[rw], s61 offen sc1\n"
"buffer_store_dwordx4 v[44:47], v0, %[rw], s61 offen offset:1024 sc1\n"
"buffer_store_dwordx4 v[44:47], v0, %[rw], s61 offen offset:2048 sc1\n"
"buffer_store_dwordx4 v[44:47], v0, %[rw], s61 offen offset:3072 sc1\n"
"buffer_store_dwordx4 v[44:47], v0, %[rw], s66 offen sc1\n"
"buffer_store_dwordx4 v[44:47], v0, %[rw], s66 offen offset:1024 sc1\n"
"buffer_store_dwordx4 v[44:47], v0, %[rw], s66 offen offset:2048 sc1\n"
"buffer_store_dwordx4 v[44:47], v0, %[rw], s66 offen offset:3072 sc1\n"
"s_add_u32 s63, s60, 3\n"
"s_and_b32 s63, s63, 3\n"
"s_lshl_b32 s63, s63, 14\n"
"buffer_store_dwordx2 v[44:45], v1, %[rw], s63 offen sc1\n"
"s_add_u32 s60, s60, 1\n"
"s_and_b32 s63, s60, 63\n"
"s_cmp_lg_u32 s63, 63\n"
"s_cbranch_scc1 L_step%=\n"
"s_add_u32 s60, s60, 1\n"
"s_cmp_lt_u32 s60, s81\n"
"s_cbranch_scc1 L_layer%=\n"
"s_branch L_end%=\n"
"L_retry%=:\n"
"s_add_u32 s64, s64, 1\n"
"s_cmp_lt_u32 s64, 0x40000\n"
"s_nop 7\n"
"s_cbranch_scc1 L_poll%=\n"
"s_mov_b32 s65, 0x40000\n"
"s_branch L_ok%=\n"
"L_end%=:\n"
"s_waitcnt vmcnt(0)\n"
    : "+{v[0:7]}"(c0), "+{s[80:87]}"(sc) : [rw] "s"(rw) : "memory", "vcc", "scc", CLOB_R);
}

__global__ void __launch_bounds__(256, 1) kMain(Params p) {
  extern __shared__ __attribute__((aligned(16))) char smem[];
  const u32 plan = __builtin_amdgcn_readfirstlane(*(const GAS u32*)(p.ws + W_CTL));
  const int b = blockIdx.x, cls = (b + plan) & 7, i = b >> 3;
  const int w = __builtin_amdgcn_readfirstlane(threadIdx.x >> 6);
  if (cls < 4 && i < 16) {
    head_role(p, cls * 8 + (i & 7), i >> 3, w);
    return;
  }
  if (cls < 4 && i < 24) {
    if (w == 0) {
      reducer_role(p, cls * 8 + (i & 7));
    } else if (w == 1 && cls == 0 && i == 16) {
      u32 best[2] = {1u << 30, 1u << 30};
#pragma unroll 1
      for (int r = 0; r < 8; ++r) {
#pragma unroll
        for (int k = 0; k < 2; ++k) {
          const char* a = p.ws + W_COMM + 2 * MB - 16384 + k * 8192;
          u64 c0, c1;
          asm volatile("s_memtime %0\n\ts_waitcnt lgkmcnt(0)\n\tglobal_store_dword %2, %3, off sc1\n\ts_waitcnt vmcnt(0)\n\ts_memtime %1\n\ts_waitcnt lgkmcnt(0)" : "=&s"(c0), "=&s"(c1) : "v"(a), "v"(0xffffffffu) : "memory");
          best[k] = min(best[k], (u32)(c1 - c0));
        }
      }
      post(mkrsrc(p.ws, W_END), F_PAR, best[1] < best[0], fresh_lane());
    }
    return;
  }
  if (cls < 4) { a_role(p, (LAS char*)smem, cls * 8 + (i & 7), w); return; }
  if (cls == 7 && i == 0 && w == 0) LK(p, 1);
  b_role(p, (LAS char*)smem, (cls - 4) * 32 + i, w);
}

extern "C" void kernel_launch(void* const* d_in, const int* in_sizes, int n_in, void* d_out, int out_size, void* d_ws, size_t ws_size, hipStream_t stream) {
  Params p;
  p.x = (const u16*)d_in[0]; p.wqkv = (const u16*)d_in[1]; p.wo = (const u16*)d_in[2]; p.mem0 = (const u16*)d_in[4];
  p.Wq = (const u16*)d_in[5]; p.Wk = (const u16*)d_in[6]; p.Wv = (const u16*)d_in[7]; p.Wo = (const u16*)d_in[8];
  p.out = (u16*)d_out; p.ws = (char*)(((size_t)d_ws + 2 * MB - 1) & ~(size_t)(2 * MB - 1));
  (void)hipFuncSetAttribute((const void*)kMain, hipFuncAttributeMaxDynamicSharedMemorySize, H_LDS);
  (void)hipFuncSetAttribute((const void*)kFirst, hipFuncAttributeMaxDynamicSharedMemorySize, 118784);
  kFirst<<<256, 256, 118784, stream>>>(p);
  kMain<<<256, 256, H_LDS, stream>>>(p);
}
```
